```python
import torch

def _softmax(x, axis=-1):
    return torch.softmax(x, dim=axis)
def _gelu(x):
    return torch.nn.functional.gelu(x, approximate="tanh")
def _silu(x):
    return torch.nn.functional.silu(x)
def _sigmoid(x):
    return torch.sigmoid(x)
def _layernorm(x, axis=-1, eps=1e-5):
    m = x.mean(dim=axis, keepdim=True)
    v = x.var(dim=axis, keepdim=True, unbiased=False)
    return (x - m) / torch.sqrt(v + eps)
def _scatter_add(tgt, idx, src, axis=0):
    return tgt.clone().index_add_(axis, idx, src)
def _segment_sum(src, seg, n):
    out = torch.zeros((n,) + src.shape[1:], dtype=src.dtype)
    return out.index_add_(0, seg, src)
def _pad(x, pw):
    flat = tuple(v for pair in reversed(pw) for v in pair)
    return torch.nn.functional.pad(x, flat)

kernel_name = 'recurrent_scan__gqa_block_cached__glu_gated_geglu__decode'

def setup_inputs():

    _w = (lambda a: (lambda t: t.to(torch.bfloat16) if t.is_floating_point() else t)(torch.as_tensor(a)))
    def _A_setup():
        B, Sq, D = 4, 1, 1024
        Hq, Hkv, Sk, Dh = 32, 4, 8192, 128
        assert Hq % Hkv == 0, (Hq, Hkv)
        return {
            "x": _w((lambda *s: torch.randn(*s, dtype=torch.bfloat16) * 0.4)(B, Sq, D)),
            "k_cache": _w((lambda *s: torch.randn(*s, dtype=torch.bfloat16) * 1.6)(B, Hkv, Sk, Dh)),
            "v_cache": _w((lambda *s: torch.randn(*s, dtype=torch.bfloat16) * 1.6)(B, Hkv, Sk, Dh)),
            "wq": _w((lambda *s: torch.randn(*s, dtype=torch.bfloat16) * 1.6)(D, Hq * Dh) * (D ** -0.5)),
            "wo": _w((lambda *s: torch.randn(*s, dtype=torch.bfloat16) * 1.6)(Hq * Dh, D) * ((Hq * Dh) ** -0.5)),
        }
    _A_in = _A_setup()

    def _B_setup():
        B, S, Dm, Df = 4, 1, 1024, 3584
        return {
            "x": _w((lambda *s: torch.randn(*s, dtype=torch.bfloat16) * 0.4)(B, S, Dm)),
            "w_gate": _w((lambda *s: torch.randn(*s, dtype=torch.bfloat16) * 1.6)(Dm, Df) * (Dm ** -0.5)),
            "w_up": _w((lambda *s: torch.randn(*s, dtype=torch.bfloat16) * 1.6)(Dm, Df) * (Dm ** -0.5)),
            "w_down": _w((lambda *s: torch.randn(*s, dtype=torch.bfloat16) * 1.6)(Df, Dm) * (Df ** -0.5)),
        }
    _B_in = _B_setup()

    return {
        "A_x": _A_in["x"],
        "A_k_cache": _A_in["k_cache"],
        "A_v_cache": _A_in["v_cache"],
        "A_wq": _A_in["wq"],
        "A_wo": _A_in["wo"],
        "B_x": _B_in["x"],
        "B_w_gate": _B_in["w_gate"],
        "B_w_up": _B_in["w_up"],
        "B_w_down": _B_in["w_down"],
    }

def reference(A_x, A_k_cache, A_v_cache, A_wq, A_wo, B_x, B_w_gate, B_w_up, B_w_down):
    def _A_ref(x, k_cache, v_cache, wq, wo):
        B, Sq, D = x.shape
        Hq, Dh = 32, 128
        Hkv = k_cache.shape[1]
        rep = Hq // Hkv
        q = (lambda x, *d: x.permute(*d))((x @ wq).reshape(B, Sq, Hq, Dh), 0, 2, 1, 3)
        k = (lambda x, n, axis: torch.repeat_interleave(x, n, dim=axis))(k_cache, rep, axis=1)
        v = (lambda x, n, axis: torch.repeat_interleave(x, n, dim=axis))(v_cache, rep, axis=1)
        scores = torch.einsum("bhqd,bhkd->bhqk", q, k) * (Dh ** -0.5)
        p = _softmax(scores, axis=-1)
        o = torch.einsum("bhqk,bhkd->bhqd", p, v)
        return (lambda x, *d: x.permute(*d))(o, 0, 2, 1, 3).reshape(B, Sq, Hq * Dh) @ wo

    def _B_ref(x, w_gate, w_up, w_down):
        g = torch.einsum("bsd,df->bsf", x, w_gate)
        u = torch.einsum("bsd,df->bsf", x, w_up)
        h = _gelu(g) * u
        return torch.einsum("bsf,fd->bsd", h, w_down)

    x = A_x
    xn = _layernorm(x, axis=-1)
    s = _A_ref(xn, A_k_cache, A_v_cache, A_wq, A_wo)
    g = _silu(_B_ref(xn, B_w_gate, B_w_up, B_w_down))
    return x + s * g
```

```cpp
#include <hip/hip_runtime.h>
#include <hip/hip_bf16.h>
#include <stdint.h>

typedef unsigned short u16;
__device__ __forceinline__ float bf2f(u16 b) { return __uint_as_float(((unsigned)b) << 16); }
__device__ __forceinline__ u16 f2bf(float f) { unsigned u = __float_as_uint(f); u += 0x7fffu + ((u >> 16) & 1u); return (u16)(u >> 16); }
__device__ __forceinline__ float rbf(float f) { return bf2f(f2bf(f)); }

constexpr int B = 4, D = 1024, HQ = 32, HKV = 4, SK = 8192, DH = 128, DF = 3584, NSPLIT = 16;

constexpr size_t WS_XN = 0;
constexpr size_t WS_Q = WS_XN + B * D;
constexpr size_t WS_H = WS_Q + B * HQ * DH;
constexpr size_t WS_PO = WS_H + B * DF;
constexpr size_t WS_PML = WS_PO + (size_t)B * HQ * NSPLIT * DH;
constexpr size_t WS_O = WS_PML + (size_t)B * HQ * NSPLIT * 2;

__device__ void compute_xn(const u16* x, float* xn_s  , float* red) {
  const int tid = threadIdx.x;
  for (int b = 0; b < B; ++b) {
    float s = 0.f;
    for (int i = tid; i < D; i += 256) s += bf2f(x[b * D + i]);
    red[tid] = s; __syncthreads();
    for (int o = 128; o > 0; o >>= 1) { if (tid < o) red[tid] += red[tid + o]; __syncthreads(); }
    const float mean = red[0] / D; __syncthreads();
    float v = 0.f;
    for (int i = tid; i < D; i += 256) { float d = bf2f(x[b * D + i]) - mean; v += d * d; }
    red[tid] = v; __syncthreads();
    for (int o = 128; o > 0; o >>= 1) { if (tid < o) red[tid] += red[tid + o]; __syncthreads(); }
    const float var = red[0] / D; __syncthreads();
    const float rs = 1.0f / sqrtf(var + 1e-5f);
    for (int i = tid; i < D; i += 256) xn_s[b * D + i] = rbf((bf2f(x[b * D + i]) - mean) * rs);
  }
  __syncthreads();
}

__global__ void __launch_bounds__(256) k1(const u16* x, const u16* wq, const u16* wg, const u16* wu, float* ws) {
  __shared__ float xn[B * D];
  __shared__ float red[256];
  compute_xn(x, xn, red);
  const int col = blockIdx.x * 64 + (threadIdx.x & 63);
  const int kq = threadIdx.x >> 6;
  __shared__ float part[4][2][B][64];
  if (blockIdx.x < 64) {
    float a[B] = {0, 0, 0, 0};
    for (int k = kq * 256; k < kq * 256 + 256; ++k) { float w = bf2f(wq[(size_t)k * 4096 + col]); for (int b = 0; b < B; ++b) a[b] += xn[b * D + k] * w; }
    for (int b = 0; b < B; ++b) part[kq][0][b][threadIdx.x & 63] = a[b];
    __syncthreads();
    if (kq == 0) for (int b = 0; b < B; ++b) { int c = threadIdx.x & 63; ws[WS_Q + b * 4096 + col] = rbf(part[0][0][b][c] + part[1][0][b][c] + part[2][0][b][c] + part[3][0][b][c]); }
  } else {
    const int c2 = col - 4096;
    float a[B] = {0, 0, 0, 0}, u[B] = {0, 0, 0, 0};
    for (int k = kq * 256; k < kq * 256 + 256; ++k) { float w = bf2f(wg[(size_t)k * DF + c2]); float w2 = bf2f(wu[(size_t)k * DF + c2]); for (int b = 0; b < B; ++b) { a[b] += xn[b * D + k] * w; u[b] += xn[b * D + k] * w2; } }
    for (int b = 0; b < B; ++b) { part[kq][0][b][threadIdx.x & 63] = a[b]; part[kq][1][b][threadIdx.x & 63] = u[b]; }
    __syncthreads();
    if (kq == 0) for (int b = 0; b < B; ++b) {
      int c = threadIdx.x & 63;
      float g = part[0][0][b][c] + part[1][0][b][c] + part[2][0][b][c] + part[3][0][b][c];
      float uu = part[0][1][b][c] + part[1][1][b][c] + part[2][1][b][c] + part[3][1][b][c];
      float t = tanhf(0.7978845608028654f * (g + 0.044715f * g * g * g));
      float ge = 0.5f * g * (1.f + t);
      ws[WS_H + b * DF + c2] = rbf(ge * uu);
    }
  }
}

__global__ void __launch_bounds__(256) k2(const u16* kc, const u16* vc, float* ws) {
  const int s = blockIdx.x, h = blockIdx.y, b = blockIdx.z, kvh = h / 8, tid = threadIdx.x;
  __shared__ float q[DH];
  __shared__ float sc[512];
  __shared__ float red[256];
  if (tid < DH) q[tid] = ws[WS_Q + b * 4096 + h * DH + tid];
  __syncthreads();
  const u16* kb = kc + ((size_t)(b * HKV + kvh) * SK + s * 512) * DH;
  const u16* vb = vc + ((size_t)(b * HKV + kvh) * SK + s * 512) * DH;
  float m = -INFINITY;
  for (int j = tid; j < 512; j += 256) {
    float d = 0.f;
    for (int i = 0; i < DH; ++i) d += q[i] * bf2f(kb[(size_t)j * DH + i]);
    d *= 0.08838834764831845f;
    sc[j] = d; m = fmaxf(m, d);
  }
  red[tid] = m; __syncthreads();
  for (int o = 128; o > 0; o >>= 1) { if (tid < o) red[tid] = fmaxf(red[tid], red[tid + o]); __syncthreads(); }
  m = red[0]; __syncthreads();
  float l = 0.f;
  for (int j = tid; j < 512; j += 256) { float p = __expf(sc[j] - m); sc[j] = p; l += p; }
  red[tid] = l; __syncthreads();
  for (int o = 128; o > 0; o >>= 1) { if (tid < o) red[tid] += red[tid + o]; __syncthreads(); }
  l = red[0]; __syncthreads();
  const int d = tid & 127, half = tid >> 7;
  float acc = 0.f;
  for (int j = half * 256; j < half * 256 + 256; ++j) acc += sc[j] * bf2f(vb[(size_t)j * DH + d]);
  red[tid] = acc; __syncthreads();
  const size_t slot = ((size_t)(b * HQ + h) * NSPLIT + s);
  if (tid < 128) ws[WS_PO + slot * DH + tid] = red[tid] + red[tid + 128];
  if (tid == 0) { ws[WS_PML + slot * 2] = m; ws[WS_PML + slot * 2 + 1] = l; }
}

__global__ void __launch_bounds__(128) k3(float* ws) {
  const int row = blockIdx.x, d = threadIdx.x;
  float M = -INFINITY;
  for (int s = 0; s < NSPLIT; ++s) M = fmaxf(M, ws[WS_PML + ((size_t)row * NSPLIT + s) * 2]);
  float L = 0.f, o = 0.f;
  for (int s = 0; s < NSPLIT; ++s) {
    float w = __expf(ws[WS_PML + ((size_t)row * NSPLIT + s) * 2] - M);
    L += w * ws[WS_PML + ((size_t)row * NSPLIT + s) * 2 + 1];
    o += w * ws[WS_PO + ((size_t)row * NSPLIT + s) * DH + d];
  }
  ws[WS_O + row * DH + d] = rbf(o / L);
}

__global__ void __launch_bounds__(256) k4(const u16* x, const u16* wo, const u16* wd, const float* ws, u16* out) {
  const int c = threadIdx.x & 15, kg = threadIdx.x >> 4;
  const int col = blockIdx.x * 16 + c;
  float s[B] = {0, 0, 0, 0}, y[B] = {0, 0, 0, 0};
  for (int k = kg; k < 4096; k += 16) { float w = bf2f(wo[(size_t)k * D + col]); for (int b = 0; b < B; ++b) s[b] += ws[WS_O + b * 4096 + k] * w; }
  for (int k = kg; k < DF; k += 16) { float w = bf2f(wd[(size_t)k * D + col]); for (int b = 0; b < B; ++b) y[b] += ws[WS_H + b * DF + k] * w; }
  __shared__ float ps[16][B][16], py[16][B][16];
  for (int b = 0; b < B; ++b) { ps[kg][b][c] = s[b]; py[kg][b][c] = y[b]; }
  __syncthreads();
  if (threadIdx.x < 64) {
    const int b = threadIdx.x >> 4;
    float ss = 0.f, yy = 0.f;
    for (int g = 0; g < 16; ++g) { ss += ps[g][b][c]; yy += py[g][b][c]; }
    float gt = yy / (1.f + __expf(-yy));
    out[b * D + col] = f2bf(bf2f(x[b * D + col]) + ss * gt);
  }
}

extern "C" void kernel_launch(void* const* d_in, const int* in_sizes, int n_in, void* d_out, int out_size, void* d_ws, size_t ws_size, hipStream_t stream) {
  const u16* x = (const u16*)d_in[0];
  const u16* kc = (const u16*)d_in[1];
  const u16* vc = (const u16*)d_in[2];
  const u16* wq = (const u16*)d_in[3];
  const u16* wo = (const u16*)d_in[4];
  const u16* wg = (const u16*)d_in[6];
  const u16* wu = (const u16*)d_in[7];
  const u16* wd = (const u16*)d_in[8];
  float* ws = (float*)d_ws;
  k1<<<64 + 56, 256, 0, stream>>>(x, wq, wg, wu, ws);
  k2<<<dim3(NSPLIT, HQ, B), 256, 0, stream>>>(kc, vc, ws);
  k3<<<B * HQ, 128, 0, stream>>>(ws);
  k4<<<64, 256, 0, stream>>>(x, wo, wd, ws, (u16*)d_out);
}
```

```cpp
#include <hip/hip_runtime.h>
#include <hip/hip_bf16.h>
#include <stdint.h>

typedef unsigned short u16;
__device__ __forceinline__ float bf2f(u16 b) { return __uint_as_float(((unsigned)b) << 16); }
__device__ __forceinline__ u16 f2bf(float f) { unsigned u = __float_as_uint(f); u += 0x7fffu + ((u >> 16) & 1u); return (u16)(u >> 16); }
__device__ __forceinline__ float rbf(float f) { return bf2f(f2bf(f)); }

constexpr int B = 4, D = 1024, HQ = 32, HKV = 4, SK = 8192, DH = 128, DF = 3584, NSPLIT = 16;

constexpr size_t WS_XN = 0;
constexpr size_t WS_Q = WS_XN + B * D;
constexpr size_t WS_H = WS_Q + B * HQ * DH;
constexpr size_t WS_PO = WS_H + B * DF;
constexpr size_t WS_PML = WS_PO + (size_t)B * HQ * NSPLIT * DH;
constexpr size_t WS_O = WS_PML + (size_t)B * HQ * NSPLIT * 2;

__device__ void compute_xn(const u16* x, float* xn_s  , float* red) {
  const int tid = threadIdx.x;
  for (int b = 0; b < B; ++b) {
    float s = 0.f;
    for (int i = tid; i < D; i += 256) s += bf2f(x[b * D + i]);
    red[tid] = s; __syncthreads();
    for (int o = 128; o > 0; o >>= 1) { if (tid < o) red[tid] += red[tid + o]; __syncthreads(); }
    const float mean = red[0] / D; __syncthreads();
    float v = 0.f;
    for (int i = tid; i < D; i += 256) { float d = bf2f(x[b * D + i]) - mean; v += d * d; }
    red[tid] = v; __syncthreads();
    for (int o = 128; o > 0; o >>= 1) { if (tid < o) red[tid] += red[tid + o]; __syncthreads(); }
    const float var = red[0] / D; __syncthreads();
    const float mb = rbf(mean), vb = rbf(var);
    const float sd = rbf(sqrtf(rbf(vb + 1e-5f)));
    for (int i = tid; i < D; i += 256) xn_s[b * D + i] = rbf(rbf(bf2f(x[b * D + i]) - mb) / sd);
  }
  __syncthreads();
}

__global__ void __launch_bounds__(256) k1(const u16* x, const u16* wq, const u16* wg, const u16* wu, float* ws) {
  __shared__ float xn[B * D];
  __shared__ float red[256];
  compute_xn(x, xn, red);
  const int col = blockIdx.x * 64 + (threadIdx.x & 63);
  const int kq = threadIdx.x >> 6;
  __shared__ float part[4][2][B][64];
  if (blockIdx.x < 64) {
    float a[B] = {0, 0, 0, 0};
    for (int k = kq * 256; k < kq * 256 + 256; ++k) { float w = bf2f(wq[(size_t)k * 4096 + col]); for (int b = 0; b < B; ++b) a[b] += xn[b * D + k] * w; }
    for (int b = 0; b < B; ++b) part[kq][0][b][threadIdx.x & 63] = a[b];
    __syncthreads();
    if (kq == 0) for (int b = 0; b < B; ++b) { int c = threadIdx.x & 63; ws[WS_Q + b * 4096 + col] = rbf(part[0][0][b][c] + part[1][0][b][c] + part[2][0][b][c] + part[3][0][b][c]); }
  } else {
    const int c2 = col - 4096;
    float a[B] = {0, 0, 0, 0}, u[B] = {0, 0, 0, 0};
    for (int k = kq * 256; k < kq * 256 + 256; ++k) { float w = bf2f(wg[(size_t)k * DF + c2]); float w2 = bf2f(wu[(size_t)k * DF + c2]); for (int b = 0; b < B; ++b) { a[b] += xn[b * D + k] * w; u[b] += xn[b * D + k] * w2; } }
    for (int b = 0; b < B; ++b) { part[kq][0][b][threadIdx.x & 63] = a[b]; part[kq][1][b][threadIdx.x & 63] = u[b]; }
    __syncthreads();
    if (kq == 0) for (int b = 0; b < B; ++b) {
      int c = threadIdx.x & 63;
      float g = part[0][0][b][c] + part[1][0][b][c] + part[2][0][b][c] + part[3][0][b][c];
      float uu = part[0][1][b][c] + part[1][1][b][c] + part[2][1][b][c] + part[3][1][b][c];
      g = rbf(g); uu = rbf(uu);
      float t = tanhf(0.7978845608028654f * (g + 0.044715f * g * g * g));
      float ge = rbf(0.5f * g * (1.f + t));
      ws[WS_H + b * DF + c2] = rbf(ge * uu);
    }
  }
}

__global__ void __launch_bounds__(256) k2(const u16* kc, const u16* vc, float* ws) {
  const int s = blockIdx.x, h = blockIdx.y, b = blockIdx.z, kvh = h / 8, tid = threadIdx.x;
  __shared__ float q[DH];
  __shared__ float sc[512];
  __shared__ float red[256];
  if (tid < DH) q[tid] = ws[WS_Q + b * 4096 + h * DH + tid];
  __syncthreads();
  const u16* kb = kc + ((size_t)(b * HKV + kvh) * SK + s * 512) * DH;
  const u16* vb = vc + ((size_t)(b * HKV + kvh) * SK + s * 512) * DH;
  float m = -INFINITY;
  for (int j = tid; j < 512; j += 256) {
    float d = 0.f;
    for (int i = 0; i < DH; ++i) d += q[i] * bf2f(kb[(size_t)j * DH + i]);
    d = rbf(rbf(d) * 0.08838834764831845f);
    sc[j] = d; m = fmaxf(m, d);
  }
  red[tid] = m; __syncthreads();
  for (int o = 128; o > 0; o >>= 1) { if (tid < o) red[tid] = fmaxf(red[tid], red[tid + o]); __syncthreads(); }
  m = red[0]; __syncthreads();
  float l = 0.f;
  for (int j = tid; j < 512; j += 256) { float p = __expf(sc[j] - m); sc[j] = p; l += p; }
  red[tid] = l; __syncthreads();
  for (int o = 128; o > 0; o >>= 1) { if (tid < o) red[tid] += red[tid + o]; __syncthreads(); }
  l = red[0]; __syncthreads();
  const int d = tid & 127, half = tid >> 7;
  float acc = 0.f;
  for (int j = half * 256; j < half * 256 + 256; ++j) acc += sc[j] * bf2f(vb[(size_t)j * DH + d]);
  red[tid] = acc; __syncthreads();
  const size_t slot = ((size_t)(b * HQ + h) * NSPLIT + s);
  if (tid < 128) ws[WS_PO + slot * DH + tid] = red[tid] + red[tid + 128];
  if (tid == 0) { ws[WS_PML + slot * 2] = m; ws[WS_PML + slot * 2 + 1] = l; }
}

__global__ void __launch_bounds__(128) k3(float* ws) {
  const int row = blockIdx.x, d = threadIdx.x;
  float M = -INFINITY;
  for (int s = 0; s < NSPLIT; ++s) M = fmaxf(M, ws[WS_PML + ((size_t)row * NSPLIT + s) * 2]);
  float L = 0.f, o = 0.f;
  for (int s = 0; s < NSPLIT; ++s) {
    float w = __expf(ws[WS_PML + ((size_t)row * NSPLIT + s) * 2] - M);
    L += w * ws[WS_PML + ((size_t)row * NSPLIT + s) * 2 + 1];
    o += w * ws[WS_PO + ((size_t)row * NSPLIT + s) * DH + d];
  }
  ws[WS_O + row * DH + d] = rbf(o / L);
}

__global__ void __launch_bounds__(256) k4(const u16* x, const u16* wo, const u16* wd, const float* ws, u16* out) {
  const int c = threadIdx.x & 15, kg = threadIdx.x >> 4;
  const int col = blockIdx.x * 16 + c;
  float s[B] = {0, 0, 0, 0}, y[B] = {0, 0, 0, 0};
  for (int k = kg; k < 4096; k += 16) { float w = bf2f(wo[(size_t)k * D + col]); for (int b = 0; b < B; ++b) s[b] += ws[WS_O + b * 4096 + k] * w; }
  for (int k = kg; k < DF; k += 16) { float w = bf2f(wd[(size_t)k * D + col]); for (int b = 0; b < B; ++b) y[b] += ws[WS_H + b * DF + k] * w; }
  __shared__ float ps[16][B][16], py[16][B][16];
  for (int b = 0; b < B; ++b) { ps[kg][b][c] = s[b]; py[kg][b][c] = y[b]; }
  __syncthreads();
  if (threadIdx.x < 64) {
    const int b = threadIdx.x >> 4;
    float ss = 0.f, yy = 0.f;
    for (int g = 0; g < 16; ++g) { ss += ps[g][b][c]; yy += py[g][b][c]; }
    ss = rbf(ss); yy = rbf(yy);
    float gt = rbf(yy / (1.f + __expf(-yy)));
    out[b * D + col] = f2bf(bf2f(x[b * D + col]) + rbf(ss * gt));
  }
}

extern "C" void kernel_launch(void* const* d_in, const int* in_sizes, int n_in, void* d_out, int out_size, void* d_ws, size_t ws_size, hipStream_t stream) {
  const u16* x = (const u16*)d_in[0];
  const u16* kc = (const u16*)d_in[1];
  const u16* vc = (const u16*)d_in[2];
  const u16* wq = (const u16*)d_in[3];
  const u16* wo = (const u16*)d_in[4];
  const u16* wg = (const u16*)d_in[6];
  const u16* wu = (const u16*)d_in[7];
  const u16* wd = (const u16*)d_in[8];
  float* ws = (float*)d_ws;
  k1<<<64 + 56, 256, 0, stream>>>(x, wq, wg, wu, ws);
  k2<<<dim3(NSPLIT, HQ, B), 256, 0, stream>>>(kc, vc, ws);
  k3<<<B * HQ, 128, 0, stream>>>(ws);
  k4<<<64, 256, 0, stream>>>(x, wo, wd, ws, (u16*)d_out);
}
```

```cpp
#include <hip/hip_runtime.h>
#include <stdint.h>

#define GAS __attribute__((address_space(1)))
#define LAS __attribute__((address_space(3)))
typedef unsigned short u16;
typedef unsigned v4u __attribute__((ext_vector_type(4)));
typedef unsigned v2u __attribute__((ext_vector_type(2)));
typedef float f32x4 __attribute__((ext_vector_type(4)));
typedef short s16x4 __attribute__((ext_vector_type(4)));
typedef __bf16 bf16x8 __attribute__((ext_vector_type(8)));

namespace fk {

constexpr int NCU = 256, NMLP = 224;
constexpr int NSLOT = 9, SLOT = 16384, RING = NSLOT * SLOT;
constexpr int XN_OFF = RING, Q_OFF = XN_OFF + 2048, P_OFF = Q_OFF + 2048, H_OFF = P_OFF + 8192, O_OFF = H_OFF + 512;
constexpr int MAX_OFF = O_OFF + 1024, SUM_OFF = MAX_OFF + 256, FLG_OFF = SUM_OFF + 256, STAMP_OFF = FLG_OFF + 256;
constexpr int LDS_BYTES = 163840;
static_assert(STAMP_OFF + 192 * 8 <= LDS_BYTES, "lds");
constexpr int F_FULL = 0, F_EPOCH = 1, F_QRDY = 2, F_ORDY = 3, F_XN = 4, F_MAX = 5, F_P = 6, F_H = 7, F_PUB1 = 9, F_PUB2 = 10, F_PUB3 = 11, F_FREE = 16;
constexpr int WS_EPOCH = 0;
constexpr int WS_QP = 4096;
constexpr int WS_Q = WS_QP + 256 * 4096;
constexpr int AP_STRIDE = 8192 + 256;
constexpr int WS_AP = WS_Q + 65536;
constexpr int WS_O = WS_AP + 256 * AP_STRIDE;
constexpr int WS_GP = WS_O + 65536;
constexpr int WS_FP = WS_GP + 56 * 16384;
constexpr int FP_STRIDE = 88 * 128;
constexpr int WS_DBG = WS_FP + 256 * FP_STRIDE;
constexpr int WS_END = WS_DBG + 256 * 192 * 8;
#define FK_W 16
constexpr int WIN = FK_W;
#define FK_DRY 0
constexpr int NWAVES = FK_DRY ? 12 : 10;

struct Params {
  const u16* x; const u16* kc; const u16* vc; const u16* wq; const u16* wo; const u16* wg; const u16* wu; const u16* wd;
  u16* out; unsigned char* ws;
};

__device__ __forceinline__ float bf2f(unsigned b) { return __uint_as_float(b << 16); }
__device__ __forceinline__ unsigned f2bf(float f) { unsigned u = __float_as_uint(f); u += 0x7fffu + ((u >> 16) & 1u); return u >> 16; }
__device__ __forceinline__ float rbf(float f) { return __uint_as_float(f2bf(f) << 16); }
__device__ __forceinline__ f32x4 mfma(v4u a, v4u b, f32x4 c) {
  return __builtin_amdgcn_mfma_f32_16x16x32_bf16(__builtin_bit_cast(bf16x8, a), __builtin_bit_cast(bf16x8, b), c, 0, 0, 0);
}
__device__ __forceinline__ v2u trread(LAS unsigned char* p) {
  return __builtin_bit_cast(v2u, __builtin_amdgcn_ds_read_tr16_b64_v4i16((LAS s16x4*)p));
}
__device__ __forceinline__ v4u trfrag(LAS unsigned char* p, int off_t1) {
  const v2u a = trread(p), b = trread(p + off_t1);
  return v4u{a.x, a.y, b.x, b.y};
}
__device__ __forceinline__ v4u ldsr128(LAS unsigned char* p) { return *(LAS v4u*)p; }
#define LGKM0() asm volatile("s_waitcnt lgkmcnt(0)" ::: "memory")
#define CBAR() asm volatile("" ::: "memory")
template <int N> __device__ __forceinline__ void waitvm() { asm volatile("s_waitcnt vmcnt(%0)" ::"n"(N) : "memory"); }
#define GLDS(src, dst) __builtin_amdgcn_global_load_lds((const unsigned*)(src), (LAS unsigned*)(dst), 16, 0, 2)

__device__ __forceinline__ float shx(float v, int m) { return __shfl_xor(v, m, 64); }

__device__ __forceinline__ void lds_wait_ge(volatile LAS unsigned* flg, int idx, unsigned want) {
#pragma unroll 1
  for (unsigned it = 0; it < (1u << 17); ++it) {
    if ((unsigned)__builtin_amdgcn_readfirstlane(flg[idx]) >= want) break;
    __builtin_amdgcn_s_sleep(1);
  }
  CBAR();
}
__device__ __forceinline__ void lds_add(volatile LAS unsigned* flg, int idx, unsigned v, int lw) {
  LGKM0();
  if (lw == 0) __hip_atomic_fetch_add((LAS unsigned*)&flg[idx], v, __ATOMIC_RELAXED, __HIP_MEMORY_SCOPE_WORKGROUP);
  CBAR();
}
__device__ __forceinline__ void gst(__amdgpu_buffer_rsrc_t rs, int off, unsigned a, unsigned b, unsigned tA, unsigned tB) {
  __builtin_amdgcn_raw_buffer_store_b128(v4u{a, tA, b, tB}, rs, off, 0, 16);
}
__device__ __forceinline__ v4u gld(__amdgpu_buffer_rsrc_t rs, int off) { return __builtin_amdgcn_raw_buffer_load_b128(rs, off, 0, 16); }
template <int N>
__device__ __forceinline__ void gather(__amdgpu_buffer_rsrc_t rs, int off, int stride, int off_last, v4u (&v)[N], unsigned tA, unsigned tB, unsigned lim) {
  unsigned todo = lim > 1u ? (1u << N) - 1u : 0u;
#pragma unroll
  for (int k = 0; k < N; ++k) v[k] = v4u{0u, 0u, 0u, 0u};
#pragma unroll 1
  for (unsigned it = 0; it < lim; ++it) {
    CBAR();
#pragma unroll
    for (int k = 0; k < N; ++k) if (todo & (1u << k)) v[k] = gld(rs, k == N - 1 ? off_last : off + k * stride);
#pragma unroll
    for (int k = 0; k < N; ++k) if (__all((v[k].y == tA) & (v[k].w == tB))) todo &= ~(1u << k);
    if (todo == 0u) break;
    __builtin_amdgcn_s_sleep(2);
  }
}

#define ST(i) do {} while (0)
#define STD(i) do {} while (0)
#define STR(i) do {} while (0)
#define DUMP(lo, n) do {} while (0)

__global__ void __launch_bounds__(64 * NWAVES) fused_kernel(Params p) {
  extern __shared__ __attribute__((aligned(16))) unsigned char smem[];
  LAS unsigned char* L = (LAS unsigned char*)smem;
  volatile LAS unsigned* flg = (volatile LAS unsigned*)(L + FLG_OFF);
  const int tid = threadIdx.x, lane = tid & 63, wave = __builtin_amdgcn_readfirstlane(tid >> 6);
  const int c = blockIdx.x;
  const bool heavy = c < NMLP;
  const int ks = c & 3, cg = c >> 2;

  if (wave == 0) {
    const int lw = lane;
    __builtin_amdgcn_s_setprio(3);
    int n = 0;
    unsigned ldsoff = 0, pub = 0;
#pragma unroll 1
    for (int seg = 0; seg < 6; ++seg) {
      const char* base; unsigned step, voff; int cnt; long long d0 = 0, d1 = 0;
      const unsigned c128 = ((lane & 7) ^ (((lane >> 4) & 1) << 1)) * 16, c256 = ((lane & 15) ^ ((lane >> 4) << 1)) * 16;
      if (seg == 0) { base = (const char*)p.wq + (size_t)(256 * ks) * 8192 + cg * 128; step = 8 * 8192; voff = (lane >> 3) * 8192 + c128; cnt = 4; }
      else if (seg == 1) { base = (const char*)p.kc + (size_t)c * 131072; step = 1024; voff = (lane >> 4) * 256 + ((lane & 15) ^ ((lane >> 4) << 2)) * 16; cnt = 16; }
      else if (seg == 2) { base = (const char*)p.vc + (size_t)c * 131072; step = 1024; voff = (lane >> 4) * 256 + c256; cnt = 16; }
      else if (seg == 3) {
        base = (const char*)p.wg + (size_t)(256 * ks) * 7168 + cg * 128; step = 8 * 7168; voff = (lane >> 3) * 7168 + c128; cnt = heavy ? 8 : 0;
        d1 = (const char*)p.wg - (const char*)p.wu; d0 = -d1 - 64 * 7168;
      }
      else if (seg == 4) { base = (const char*)p.wo + (size_t)(128 * (c >> 3)) * 2048 + (c & 7) * 256; step = 4 * 2048; voff = (lane >> 4) * 2048 + c256; cnt = 4; }
      else { base = (const char*)p.wd + (size_t)(64 * cg) * 2048 + ks * 512; step = 2 * 2048; voff = (lane >> 5) * 2048 + ((lane & 31) ^ ((lane >> 5) << 1)) * 16; cnt = heavy ? 4 : 0; }
#pragma unroll 1
      for (int i = 0; i < cnt; ++i) {
#pragma unroll
        for (int j = 0; j < 8; ++j) { GLDS(base + voff, L + (ldsoff + j * 1024)); base += step; }
        base += (i & 1) ? d1 : d0;
        ldsoff += 8192; if (ldsoff == RING) ldsoff = 0;
        ++n;
        if (pub >= 2 && !(pub & 1)) ST((pub >> 1) - 1);
        if (n == WIN / 8 + 1) __builtin_amdgcn_s_barrier();
        if (n >= 2 * NSLOT && !(n & 1)) {
          const int fp = (n >> 1) - NSLOT;
          if ((unsigned)__builtin_amdgcn_readfirstlane(flg[F_FREE + fp]) < 8u) {
            waitvm<0>();
            pub = (unsigned)n; flg[F_FULL] = pub;
            lds_wait_ge(flg, F_FREE + fp, 8u);
          }
        }
        if (n > WIN / 8) {
          waitvm<WIN>();
          if (pub < (unsigned)(n - WIN / 8)) { pub = (unsigned)(n - WIN / 8); flg[F_FULL] = pub; }
        }
      }
    }
    if (WIN >= 32) { waitvm<24>(); if (pub < (unsigned)(n - 3)) { pub = (unsigned)(n - 3); flg[F_FULL] = pub; } }
    if (WIN >= 24) { waitvm<16>(); if (pub < (unsigned)(n - 2)) { pub = (unsigned)(n - 2); flg[F_FULL] = pub; } }
    ST((n >> 1) - 2);
    waitvm<8>();
    if (pub < (unsigned)(n - 1)) { pub = (unsigned)(n - 1); flg[F_FULL] = pub; }
    waitvm<0>();
    flg[F_FULL] = (unsigned)n; ST((n >> 1) - 1);
    ST(31);
    STR(29);
    DUMP(0, 32);
    return;
  }

  const __amdgpu_buffer_rsrc_t rs = __builtin_amdgcn_make_buffer_rsrc((void*)p.ws, 0, WS_END, 0x00020000);
  const bool dry = wave >= 10;
  const int lw = dry ? 4096 : lane;
  const unsigned wm = dry ? 0u : ~0u;
  const unsigned glim = dry ? 1u : (1u << 14);

  if (wave == 1 || wave == 11) {
    const unsigned E = __hip_atomic_load((GAS unsigned*)(p.ws + WS_EPOCH), __ATOMIC_RELAXED, __HIP_MEMORY_SCOPE_AGENT);
    if (lw < 64) flg[lane] = lane == F_EPOCH ? E : 0u;
    const int fj = lane & 7;
    const unsigned xa = p.x[(2 * (fj & 1)) * 1024 + 4 * c + (fj >> 1)], xb = p.x[(2 * (fj & 1) + 1) * 1024 + 4 * c + (fj >> 1)];
    LGKM0();
    __builtin_amdgcn_s_barrier();
    const unsigned tA = __builtin_amdgcn_readfirstlane(E) + 1u, tB = tA ^ 0x9E3779B9u;
    ST(32);
    lds_wait_ge(flg, F_PUB1, 8u & wm);
    ST(33);
    {
      v4u v[4];
      gather<4>(rs, WS_QP + c * 4096 + lane * 16, 1024, WS_QP + c * 4096 + lane * 16 + 3072, v, tA, tB, glim);
      float a = 0.f, b = 0.f;
#pragma unroll
      for (int i = 0; i < 4; ++i) { a += __uint_as_float(v[i].x); b += __uint_as_float(v[i].z); }
      a += shx(a, 32); b += shx(b, 32);
      const unsigned pk = f2bf(a) | (f2bf(b) << 16);
      const unsigned p0 = __builtin_amdgcn_mov_dpp(pk, 0x00, 0xf, 0xf, true), p1 = __builtin_amdgcn_mov_dpp(pk, 0x55, 0xf, 0xf, true);
      const unsigned p2 = __builtin_amdgcn_mov_dpp(pk, 0xAA, 0xf, 0xf, true), p3 = __builtin_amdgcn_mov_dpp(pk, 0xFF, 0xf, 0xf, true);
      const int e = lane & 1;
      const unsigned d0 = e ? ((p0 >> 16) | (p1 & 0xffff0000u)) : ((p0 & 0xffffu) | (p1 << 16));
      const unsigned d1 = e ? ((p2 >> 16) | (p3 & 0xffff0000u)) : ((p2 & 0xffffu) | (p3 << 16));
      if (lw < 32 && (lane & 3) < 2) {
        const int bb = 2 * (lane >> 4) + e;
        gst(rs, WS_Q + bb * 16384 + (4 * c + ((lane & 15) >> 2)) * 16, d0, d1, tA, tB);
      }
    }
    ST(34);
    {
      v4u v[4];
      const int o = WS_Q + (c >> 6) * 16384 + ((c >> 4) & 3) * 4096 + lane * 16;
      gather<4>(rs, o, 1024, o + 3072, v, tA, tB, glim);
      if (lw < 64) {
#pragma unroll
        for (int i = 0; i < 4; ++i) *(LAS v2u*)(L + Q_OFF + (64 * i + lane) * 8) = v2u{v[i].x, v[i].z};
        LGKM0();
        flg[F_QRDY] = 1u;
      }
    }
    ST(35);
    lds_wait_ge(flg, F_PUB2, 8u & wm);
    ST(36);
    {
      v4u v[9];
      gather<9>(rs, WS_AP + c * AP_STRIDE + lane * 16, 1024, WS_AP + c * AP_STRIDE + 8192 + (lane & 15) * 16, v, tA, tB, glim);
      const float m = __uint_as_float(v[8].x), l = __uint_as_float(v[8].z);
      float M = m;
      M = fmaxf(M, shx(M, 1)); M = fmaxf(M, shx(M, 2)); M = fmaxf(M, shx(M, 4)); M = fmaxf(M, shx(M, 8));
      const float w = __builtin_amdgcn_exp2f((m - M) * 1.4426950408889634f);
      float Ls = w * l;
      Ls += shx(Ls, 1); Ls += shx(Ls, 2); Ls += shx(Ls, 4); Ls += shx(Ls, 8);
      float a = 0.f, b = 0.f;
#pragma unroll
      for (int i = 0; i < 8; ++i) {
        const float w0 = __builtin_bit_cast(float, __builtin_amdgcn_readlane(__builtin_bit_cast(int, w), 2 * i));
        const float w1 = __builtin_bit_cast(float, __builtin_amdgcn_readlane(__builtin_bit_cast(int, w), 2 * i + 1));
        const float ws_ = lane < 32 ? w0 : w1;
        a = fmaf(ws_, __uint_as_float(v[i].x), a); b = fmaf(ws_, __uint_as_float(v[i].z), b);
      }
      a += shx(a, 32); b += shx(b, 32);
      const unsigned pk = f2bf(a / Ls) | (f2bf(b / Ls) << 16);
      const unsigned nb = __builtin_amdgcn_mov_dpp(pk, 0xF5, 0xf, 0xf, true);
      const int hh = ((c >> 4) & 3) * 8 + ((c & 15) >> 1), bb = c >> 6, dh = c & 1;
      if (lw < 32 && (lane & 1) == 0) gst(rs, WS_O + hh * 2048 + bb * 512 + (dh * 16 + (lane >> 1)) * 16, pk, nb, tA, tB);
    }
    ST(37);
    {
      v4u v[2];
      const int o = WS_O + (c >> 3) * 2048 + lane * 16;
      gather<2>(rs, o, 1024, o + 1024, v, tA, tB, glim);
      if (lw < 64) {
#pragma unroll
        for (int i = 0; i < 2; ++i) *(LAS v2u*)(L + O_OFF + (64 * i + lane) * 8) = v2u{v[i].x, v[i].z};
        LGKM0();
        flg[F_ORDY] = 1u;
      }
    }
    ST(38);
    lds_wait_ge(flg, F_PUB3, (heavy ? 16u : 8u) & wm);
    ST(39);
    {
      v4u v[11];
      const int o = WS_FP + c * FP_STRIDE + lane * 16;
      gather<11>(rs, o, 1024, o + 10240, v, tA, tB, glim);
      float s0 = 0.f, s1 = 0.f, y0 = 0.f, y1 = 0.f;
#pragma unroll
      for (int i = 0; i < 4; ++i) { s0 += __uint_as_float(v[i].x); s1 += __uint_as_float(v[i].z); }
#pragma unroll
      for (int i = 4; i < 11; ++i) { y0 += __uint_as_float(v[i].x); y1 += __uint_as_float(v[i].z); }
#pragma unroll
      for (int m = 8; m <= 32; m <<= 1) { s0 += shx(s0, m); s1 += shx(s1, m); y0 += shx(y0, m); y1 += shx(y1, m); }
      s0 = rbf(s0); s1 = rbf(s1); y0 = rbf(y0); y1 = rbf(y1);
      const float g0 = rbf(y0 / (1.f + __expf(-y0))), g1 = rbf(y1 / (1.f + __expf(-y1)));
      const unsigned o0 = f2bf(bf2f(xa) + rbf(s0 * g0)), o1 = f2bf(bf2f(xb) + rbf(s1 * g1));
      if (lw < 8) {
        p.out[(2 * (fj & 1)) * 1024 + 4 * c + (fj >> 1)] = (u16)o0;
        p.out[(2 * (fj & 1) + 1) * 1024 + 4 * c + (fj >> 1)] = (u16)o1;
      }
      if (c == 0 && lw == 0) __hip_atomic_store((GAS unsigned*)(p.ws + WS_EPOCH), tA, __ATOMIC_RELAXED, __HIP_MEMORY_SCOPE_AGENT);
    }
    ST(40);
    if (dry) { STD(42); }
    if (!dry) DUMP(32, 16);
    return;
  }

  const int w = dry ? 0 : wave - 2;
  const int g = lane >> 4, q4 = (lane & 15) >> 2, p4 = lane & 3;
  v4u xv0 = {0, 0, 0, 0}, xv1 = {0, 0, 0, 0};
  if (w < 4) {
    xv0 = *(const GAS v4u*)(p.x + w * 1024 + 8 * lane);
    xv1 = *(const GAS v4u*)(p.x + w * 1024 + 512 + 8 * lane);
  }
  __builtin_amdgcn_s_barrier();
  const unsigned tA = __builtin_amdgcn_readfirstlane(flg[F_EPOCH]) + 1u, tB = tA ^ 0x9E3779B9u;
  const int sb = 48 + w * 16;
  ST(sb + 0);
  if (w < 4) {
    float xf[16];
#pragma unroll
    for (int i = 0; i < 4; ++i) {
      xf[2 * i] = bf2f(xv0[i] & 0xffffu); xf[2 * i + 1] = bf2f(xv0[i] >> 16);
      xf[8 + 2 * i] = bf2f(xv1[i] & 0xffffu); xf[8 + 2 * i + 1] = bf2f(xv1[i] >> 16);
    }
    float s = 0.f;
#pragma unroll
    for (int i = 0; i < 16; ++i) s += xf[i];
#pragma unroll
    for (int m = 1; m <= 32; m <<= 1) s += shx(s, m);
    const float mean = s * (1.f / 1024.f);
    float vv = 0.f;
#pragma unroll
    for (int i = 0; i < 16; ++i) { const float d = xf[i] - mean; vv = fmaf(d, d, vv); }
#pragma unroll
    for (int m = 1; m <= 32; m <<= 1) vv += shx(vv, m);
    const float mb = rbf(mean), vb = rbf(vv * (1.f / 1024.f));
    const float sd = rbf(sqrtf(rbf(vb + 1e-5f)));
    const int hi = ks >> 1;
    unsigned o[4];
#pragma unroll
    for (int i = 0; i < 4; ++i) {
      const float a = hi ? xf[8 + 2 * i] : xf[2 * i], b = hi ? xf[8 + 2 * i + 1] : xf[2 * i + 1];
      o[i] = f2bf(rbf(a - mb) / sd) | (f2bf(rbf(b - mb) / sd) << 16);
    }
    if ((lw >> 5) == (ks & 1)) *(LAS v4u*)(L + XN_OFF + w * 512 + (lane & 31) * 16) = v4u{o[0], o[1], o[2], o[3]};
    lds_add(flg, F_XN, 1u, lw);
  }
  lds_wait_ge(flg, F_XN, 4u & wm);
  ST(sb + 1);
  const int arow = (lane & 3);
  const int trow = 8 * g + q4;
  {
    const int ct = w & 3, kh = w >> 2;
    lds_wait_ge(flg, F_FULL, (unsigned)(2 * kh + 2) & wm);
    LAS unsigned char* tb = L + kh * SLOT + trow * 128 + 32 * (ct ^ (q4 >> 1)) + 8 * p4;
    LAS unsigned char* ab = L + XN_OFF + arow * 512 + kh * 256 + 16 * g;
    f32x4 acc = {0.f, 0.f, 0.f, 0.f};
    v4u bfr[4], afr[4];
#pragma unroll
    for (int k = 0; k < 4; ++k) { bfr[k] = trfrag(tb + k * 4096, 512); afr[k] = ldsr128(ab + k * 64); }
    lds_add(flg, F_FREE + kh, 2u, lw);
#pragma unroll
    for (int k = 0; k < 4; ++k) acc = mfma(afr[k], bfr[k], acc);
    if (lw < 32) {
      const int bp = lane >> 4;
      gst(rs, WS_QP + (4 * cg + ct) * 4096 + (2 * ks + kh) * 512 + lane * 16, __float_as_uint(bp ? acc[2] : acc[0]), __float_as_uint(bp ? acc[3] : acc[1]), tA, tB);
    }
    lds_add(flg, F_PUB1, 1u, lw);
  }
  ST(sb + 2);
  float sc[16];
  {
    lds_wait_ge(flg, F_FULL, (unsigned)(2 * (2 + w + 1)) & wm);
    const int i = lane & 15;
    const int slot = (2 + w) % NSLOT;
    LAS unsigned char* kb = L + slot * SLOT + (8 * (i >> 2) + (i & 3)) * 256 + 16 * g;
    v4u kf[16];
#pragma unroll
    for (int kt = 0; kt < 4; ++kt)
#pragma unroll
      for (int s = 0; s < 4; ++s) kf[kt * 4 + s] = ldsr128(kb + ((kt >> 1) * 32 + (kt & 1) * 4) * 256 + 64 * (s ^ (i & 3)));
    lds_add(flg, F_FREE + 2 + w, 8u, lw);
    lds_wait_ge(flg, F_QRDY, 1u & wm);
    ST(sb + 3);
    v4u qf[4];
#pragma unroll
    for (int s = 0; s < 4; ++s) qf[s] = ldsr128(L + Q_OFF + (lane & 7) * 256 + 64 * s + 16 * g);
    float mx = -INFINITY;
#pragma unroll
    for (int kt = 0; kt < 4; ++kt) {
      f32x4 acc = {0.f, 0.f, 0.f, 0.f};
#pragma unroll
      for (int s = 0; s < 4; ++s) acc = mfma(kf[kt * 4 + s], qf[s], acc);
#pragma unroll
      for (int r = 0; r < 4; ++r) { const float v = rbf(rbf(acc[r]) * 0.08838834764831845f); sc[kt * 4 + r] = v; mx = fmaxf(mx, v); }
    }
    mx = fmaxf(mx, shx(mx, 16)); mx = fmaxf(mx, shx(mx, 32));
    if (lw < 8) ((LAS float*)(L + MAX_OFF))[w * 8 + lane] = mx;
    lds_add(flg, F_MAX, 1u, lw);
  }
  ST(sb + 4);
  float mcu;
  {
    lds_wait_ge(flg, F_MAX, 8u & wm);
    mcu = -INFINITY;
#pragma unroll
    for (int ww = 0; ww < 8; ++ww) mcu = fmaxf(mcu, ((LAS float*)(L + MAX_OFF))[ww * 8 + (lane & 7)]);
    float sum = 0.f;
    unsigned pk[8];
#pragma unroll
    for (int i = 0; i < 8; ++i) {
      const float a = __builtin_amdgcn_exp2f((sc[2 * i] - mcu) * 1.4426950408889634f), b = __builtin_amdgcn_exp2f((sc[2 * i + 1] - mcu) * 1.4426950408889634f);
      sum += a + b;
      pk[i] = f2bf(a) | (f2bf(b) << 16);
    }
    sum += shx(sum, 16); sum += shx(sum, 32);
    if (lw < 8) ((LAS float*)(L + SUM_OFF))[w * 8 + lane] = sum;
    if ((lw & (4096 + 8)) == 0) {
      *(LAS v4u*)(L + P_OFF + (2 * w) * 512 + g * 128 + (lane & 7) * 16) = v4u{pk[0], pk[1], pk[2], pk[3]};
      *(LAS v4u*)(L + P_OFF + (2 * w + 1) * 512 + g * 128 + (lane & 7) * 16) = v4u{pk[4], pk[5], pk[6], pk[7]};
    }
    lds_add(flg, F_P, 1u, lw);
  }
  ST(sb + 5);
  {
    lds_wait_ge(flg, F_P, 8u & wm);
    f32x4 acc = {0.f, 0.f, 0.f, 0.f};
    const int voff = trow * 256 + 32 * (w ^ q4) + 8 * p4;
    LAS unsigned char* pb = L + P_OFF + g * 128 + (lane & 7) * 16;
    int slot = (10) % NSLOT;
#pragma unroll 1
    for (int fi = 0; fi < 8; ++fi) {
      const v4u pf0 = ldsr128(pb + (2 * fi) * 512), pf1 = ldsr128(pb + (2 * fi + 1) * 512);
      lds_wait_ge(flg, F_FULL, (unsigned)(2 * (10 + fi + 1)) & wm);
      LAS unsigned char* vb = L + slot * SLOT + voff;
      const v4u a0 = trfrag(vb, 1024), a1 = trfrag(vb + 8192, 1024);
      lds_add(flg, F_FREE + 10 + fi, 1u, lw);
      acc = mfma(a0, pf0, acc);
      acc = mfma(a1, pf1, acc);
      slot = (slot + 1 == NSLOT) ? 0 : slot + 1;
    }
    ST(sb + 6);
    const int hd = lane & 7, half = (lane >> 3) & 1;
    const int grp = c >> 4, s = c & 15;
    if (lw < 64) gst(rs, WS_AP + (grp * 16 + hd * 2 + (w >> 2)) * AP_STRIDE + s * 512 + ((w & 3) * 8 + 2 * g + half) * 16,
        __float_as_uint(half ? acc[2] : acc[0]), __float_as_uint(half ? acc[3] : acc[1]), tA, tB);
    if (w == 0) {
      float lsum = 0.f;
#pragma unroll
      for (int ww = 0; ww < 8; ++ww) lsum += ((LAS float*)(L + SUM_OFF))[ww * 8 + hd];
      if (lw < 16) gst(rs, WS_AP + (grp * 16 + hd * 2 + half) * AP_STRIDE + 8192 + s * 16, __float_as_uint(mcu), __float_as_uint(lsum), tA, tB);
    }
    lds_add(flg, F_PUB2, 1u, lw);
  }
  ST(sb + 7);
  int f = 18;
  if (heavy) {
    const int mat = w >> 2, ct = w & 3;
    f32x4 acc = {0.f, 0.f, 0.f, 0.f};
    const int toff = mat * 8192 + trow * 128 + 32 * (ct ^ (q4 >> 1)) + 8 * p4;
    LAS unsigned char* ab = L + XN_OFF + arow * 512 + 16 * g;
#pragma unroll 1
    for (int fi = 0; fi < 4; ++fi) {
      const v4u a0 = ldsr128(ab + fi * 128), a1 = ldsr128(ab + fi * 128 + 64);
      lds_wait_ge(flg, F_FULL, (unsigned)(2 * (18 + fi) + 1 + mat) & wm);
      LAS unsigned char* tb = L + fi * SLOT + toff;
      const v4u b0 = trfrag(tb, 512), b1 = trfrag(tb + 4096, 512);
      acc = mfma(a0, b0, acc);
      acc = mfma(a1, b1, acc);
    }
    if (lw < 32) {
      const int bp = lane >> 4;
      gst(rs, WS_GP + cg * 16384 + ks * 4096 + w * 512 + lane * 16, __float_as_uint(bp ? acc[2] : acc[0]), __float_as_uint(bp ? acc[3] : acc[1]), tA, tB);
    }
    f = 22;
    ST(sb + 8);
    {
      v4u v[2];
      const int ct2 = w >> 1, n3 = lane & 7, bp = (lane >> 3) & 1;
      const int o = WS_GP + cg * 16384 + g * 4096 + ct2 * 512 + (bp * 16 + (w & 1) * 8 + n3) * 16;
      gather<2>(rs, o, 2048, o + 2048, v, tA, tB, glim);
      float g0 = __uint_as_float(v[0].x), g1 = __uint_as_float(v[0].z), u0 = __uint_as_float(v[1].x), u1 = __uint_as_float(v[1].z);
      g0 += shx(g0, 16); g1 += shx(g1, 16); u0 += shx(u0, 16); u1 += shx(u1, 16);
      g0 += shx(g0, 32); g1 += shx(g1, 32); u0 += shx(u0, 32); u1 += shx(u1, 32);
      g0 = rbf(g0); g1 = rbf(g1); u0 = rbf(u0); u1 = rbf(u1);
      const float z0 = 0.7978845608028654f * (g0 + 0.044715f * g0 * g0 * g0), z1 = 0.7978845608028654f * (g1 + 0.044715f * g1 * g1 * g1);
      const float t0 = 1.f - 2.f / (__expf(2.f * z0) + 1.f), t1 = 1.f - 2.f / (__expf(2.f * z1) + 1.f);
      const unsigned h0 = f2bf(rbf(0.5f * g0 * (1.f + t0)) * u0), h1 = f2bf(rbf(0.5f * g1 * (1.f + t1)) * u1);
      if (lw < 16) {
        const int fcol = 16 * ct2 + 8 * (w & 1) + n3;
        ((LAS u16*)(L + H_OFF))[(2 * bp) * 64 + fcol] = (u16)h0;
        ((LAS u16*)(L + H_OFF))[(2 * bp + 1) * 64 + fcol] = (u16)h1;
      }
      lds_add(flg, F_H, 1u, lw);
    }
    ST(sb + 9);
  }
  {
    lds_wait_ge(flg, F_ORDY, 1u & wm);
    ST(sb + 10);
    f32x4 acc = {0.f, 0.f, 0.f, 0.f};
    const int toff = trow * 256 + 32 * (w ^ q4) + 8 * p4;
    LAS unsigned char* ab = L + O_OFF + arow * 256 + 16 * g;
#pragma unroll 1
    for (int fi = 0; fi < 2; ++fi) {
      const v4u a0 = ldsr128(ab + fi * 128), a1 = ldsr128(ab + fi * 128 + 64);
      lds_wait_ge(flg, F_FULL, (unsigned)(2 * (f + fi + 1)) & wm);
      LAS unsigned char* tb = L + (f - 18 + fi) * SLOT + toff;
      const v4u b0 = trfrag(tb, 1024), b1 = trfrag(tb + 8192, 1024);
      acc = mfma(a0, b0, acc);
      acc = mfma(a1, b1, acc);
    }
    if (lw < 32) {
      const int bp = lane >> 4, n = lane & 15;
      gst(rs, WS_FP + (32 * (c & 7) + 4 * w + (n >> 2)) * FP_STRIDE + (c >> 3) * 128 + ((n & 3) * 2 + bp) * 16,
          __float_as_uint(bp ? acc[2] : acc[0]), __float_as_uint(bp ? acc[3] : acc[1]), tA, tB);
    }
    lds_add(flg, F_PUB3, 1u, lw);
  }
  ST(sb + 11);
  if (heavy) {
    lds_wait_ge(flg, F_H, 8u & wm);
    f32x4 acc0 = {0.f, 0.f, 0.f, 0.f}, acc1 = {0.f, 0.f, 0.f, 0.f};
    const int toff0 = trow * 512 + 32 * ((2 * w) ^ (q4 & 1)) + 8 * p4, toff1 = trow * 512 + 32 * ((2 * w + 1) ^ (q4 & 1)) + 8 * p4;
    LAS unsigned char* ab = L + H_OFF + arow * 128 + 16 * g;
#pragma unroll 1
    for (int fi = 0; fi < 2; ++fi) {
      const v4u a0 = ldsr128(ab + fi * 64);
      lds_wait_ge(flg, F_FULL, (unsigned)(2 * (24 + fi + 1)) & wm);
      LAS unsigned char* tb = L + (6 + fi) * SLOT;
      const v4u b0 = trfrag(tb + toff0, 2048), b1 = trfrag(tb + toff1, 2048);
      acc0 = mfma(a0, b0, acc0);
      acc1 = mfma(a0, b1, acc1);
    }
    const int bp = g & 1, tl = g >> 1, n = lane & 15;
    const float e0 = tl ? (bp ? acc1[2] : acc1[0]) : (bp ? acc0[2] : acc0[0]);
    const float e1 = tl ? (bp ? acc1[3] : acc1[1]) : (bp ? acc0[3] : acc0[1]);
    if (lw < 64) gst(rs, WS_FP + (64 * ks + 8 * w + 4 * tl + (n >> 2)) * FP_STRIDE + (32 + cg) * 128 + ((n & 3) * 2 + bp) * 16, __float_as_uint(e0), __float_as_uint(e1), tA, tB);
    lds_add(flg, F_PUB3, 1u, lw);
  }
  ST(sb + 12);
  if (dry) { STD(41); }
  if (!dry) DUMP(sb, 16);
}

}

extern "C" void kernel_launch(void* const* d_in, const int* in_sizes, int n_in, void* d_out, int out_size, void* d_ws, size_t ws_size, hipStream_t stream) {
  fk::Params p;
  p.x = (const u16*)d_in[0]; p.kc = (const u16*)d_in[1]; p.vc = (const u16*)d_in[2]; p.wq = (const u16*)d_in[3]; p.wo = (const u16*)d_in[4];
  p.wg = (const u16*)d_in[6]; p.wu = (const u16*)d_in[7]; p.wd = (const u16*)d_in[8];
  p.out = (u16*)d_out; p.ws = (unsigned char*)d_ws;
  (void)hipFuncSetAttribute((const void*)fk::fused_kernel, hipFuncAttributeMaxDynamicSharedMemorySize, fk::LDS_BYTES);
  hipLaunchKernelGGL(fk::fused_kernel, dim3(fk::NCU), dim3(64 * fk::NWAVES), fk::LDS_BYTES, stream, p);
}
```

```cpp
#include <hip/hip_runtime.h>
#include <stdint.h>

#define GAS __attribute__((address_space(1)))
#define LAS __attribute__((address_space(3)))
typedef unsigned short u16;
typedef unsigned v4u __attribute__((ext_vector_type(4)));
typedef unsigned v2u __attribute__((ext_vector_type(2)));
typedef float f32x4 __attribute__((ext_vector_type(4)));
typedef short s16x4 __attribute__((ext_vector_type(4)));
typedef __bf16 bf16x8 __attribute__((ext_vector_type(8)));

namespace fk {

constexpr int NCU = 256, NMLP = 224;
constexpr int NSLOT = 9, SLOT = 16384, RING = NSLOT * SLOT;
constexpr int XN_OFF = RING, Q_OFF = XN_OFF + 2048, P_OFF = Q_OFF + 2048, H_OFF = P_OFF + 8192, O_OFF = H_OFF + 512;
constexpr int MAX_OFF = O_OFF + 1024, SUM_OFF = MAX_OFF + 256, FLG_OFF = SUM_OFF + 256, STAMP_OFF = FLG_OFF + 256;
constexpr int LDS_BYTES = 163840;
static_assert(STAMP_OFF + 192 * 8 <= LDS_BYTES, "lds");
constexpr int F_FULL = 0, F_EPOCH = 1, F_QRDY = 2, F_ORDY = 3, F_XN = 4, F_MAX = 5, F_P = 6, F_H = 7, F_PUB1 = 9, F_PUB2 = 10, F_PUB3 = 11, F_FREE = 16;
constexpr int WS_EPOCH = 0;
constexpr int WS_QP = 4096;
constexpr int WS_Q = WS_QP + 256 * 4096;
constexpr int AP_STRIDE = 8192 + 256;
constexpr int WS_AP = WS_Q + 65536;
constexpr int WS_O = WS_AP + 256 * AP_STRIDE;
constexpr int WS_GP = WS_O + 65536;
constexpr int WS_FP = WS_GP + 56 * 16384;
constexpr int FP_STRIDE = 88 * 128;
constexpr int WS_DBG = WS_FP + 256 * FP_STRIDE;
constexpr int WS_END = WS_DBG + 256 * 192 * 8;
#define FK_W 16
constexpr int WIN = FK_W;
#define FK_DRY 0
constexpr int NWAVES = FK_DRY ? 12 : 10;

struct Params {
  const u16* x; const u16* kc; const u16* vc; const u16* wq; const u16* wo; const u16* wg; const u16* wu; const u16* wd;
  u16* out; unsigned char* ws;
};

__device__ __forceinline__ float bf2f(unsigned b) { return __uint_as_float(b << 16); }
__device__ __forceinline__ unsigned f2bf(float f) { unsigned u = __float_as_uint(f); u += 0x7fffu + ((u >> 16) & 1u); return u >> 16; }
__device__ __forceinline__ float rbf(float f) { return __uint_as_float(f2bf(f) << 16); }
__device__ __forceinline__ f32x4 mfma(v4u a, v4u b, f32x4 c) {
  return __builtin_amdgcn_mfma_f32_16x16x32_bf16(__builtin_bit_cast(bf16x8, a), __builtin_bit_cast(bf16x8, b), c, 0, 0, 0);
}
__device__ __forceinline__ v2u trread(LAS unsigned char* p) {
  return __builtin_bit_cast(v2u, __builtin_amdgcn_ds_read_tr16_b64_v4i16((LAS s16x4*)p));
}
__device__ __forceinline__ v4u trfrag(LAS unsigned char* p, int off_t1) {
  const v2u a = trread(p), b = trread(p + off_t1);
  return v4u{a.x, a.y, b.x, b.y};
}
__device__ __forceinline__ v4u ldsr128(LAS unsigned char* p) { return *(LAS v4u*)p; }
#define LGKM0() asm volatile("s_waitcnt lgkmcnt(0)" ::: "memory")
#define CBAR() asm volatile("" ::: "memory")
template <int N> __device__ __forceinline__ void waitvm() { asm volatile("s_waitcnt vmcnt(%0)" ::"n"(N) : "memory"); }
#define GLDS(src, dst) __builtin_amdgcn_global_load_lds((const unsigned*)(src), (LAS unsigned*)(dst), 16, 0, 2)

__device__ __forceinline__ float shx(float v, int m) { return __shfl_xor(v, m, 64); }

__device__ __forceinline__ void lds_wait_ge(volatile LAS unsigned* flg, int idx, unsigned want) {
#pragma unroll 1
  for (unsigned it = 0; it < (1u << 17); ++it) {
    if ((unsigned)__builtin_amdgcn_readfirstlane(flg[idx]) >= want) break;
    __builtin_amdgcn_s_sleep(1);
  }
  CBAR();
}
__device__ __forceinline__ void lds_add(volatile LAS unsigned* flg, int idx, unsigned v, int lw) {
  LGKM0();
  if (lw == 0) __hip_atomic_fetch_add((LAS unsigned*)&flg[idx], v, __ATOMIC_RELAXED, __HIP_MEMORY_SCOPE_WORKGROUP);
  CBAR();
}
__device__ __forceinline__ void gst(__amdgpu_buffer_rsrc_t rs, int off, unsigned a, unsigned b, unsigned tA, unsigned tB) {
  __builtin_amdgcn_raw_buffer_store_b128(v4u{a, tA, b, tB}, rs, off, 0, 16);
}
__device__ __forceinline__ v4u gld(__amdgpu_buffer_rsrc_t rs, int off) { return __builtin_amdgcn_raw_buffer_load_b128(rs, off, 0, 16); }
template <int N>
__device__ __forceinline__ void gather(__amdgpu_buffer_rsrc_t rs, int off, int stride, int off_last, v4u (&v)[N], unsigned tA, unsigned tB, unsigned lim) {
  unsigned todo = lim > 1u ? (1u << N) - 1u : 0u;
#pragma unroll
  for (int k = 0; k < N; ++k) v[k] = v4u{0u, 0u, 0u, 0u};
#pragma unroll 1
  for (unsigned it = 0; it < lim; ++it) {
    CBAR();
#pragma unroll
    for (int k = 0; k < N; ++k) if (todo & (1u << k)) v[k] = gld(rs, k == N - 1 ? off_last : off + k * stride);
#pragma unroll
    for (int k = 0; k < N; ++k) if (__all((v[k].y == tA) & (v[k].w == tB))) todo &= ~(1u << k);
    if (todo == 0u) break;
    __builtin_amdgcn_s_sleep(2);
  }
}

#define ST(i) do {} while (0)
#define STD(i) do {} while (0)
#define STR(i) do {} while (0)
#define DUMP(lo, n) do {} while (0)

__global__ void __launch_bounds__(64 * NWAVES) fused_kernel(Params p) {
  extern __shared__ __attribute__((aligned(16))) unsigned char smem[];
  LAS unsigned char* L = (LAS unsigned char*)smem;
  volatile LAS unsigned* flg = (volatile LAS unsigned*)(L + FLG_OFF);
  const int tid = threadIdx.x, lane = tid & 63, wave = __builtin_amdgcn_readfirstlane(tid >> 6);
  const int c = blockIdx.x;
  const bool heavy = c < NMLP;
  const int ks = c & 3, cg = c >> 2;

  if (wave == 0) {
    const int lw = lane;
    __builtin_amdgcn_s_setprio(3);
    const unsigned long long xt0 = __builtin_amdgcn_s_memrealtime();
    int n = 0;
    unsigned ldsoff = 0, pub = 0;
#pragma unroll 1
    for (int seg = 0; seg < 6; ++seg) {
      const char* base; unsigned step, voff; int cnt; long long d0 = 0, d1 = 0;
      const unsigned c128 = ((lane & 7) ^ (((lane >> 4) & 1) << 1)) * 16, c256 = ((lane & 15) ^ ((lane >> 4) << 1)) * 16;
      if (seg == 0) { base = (const char*)p.wq + (size_t)(256 * ks) * 8192 + cg * 128; step = 8 * 8192; voff = (lane >> 3) * 8192 + c128; cnt = 4; }
      else if (seg == 1) { base = (const char*)p.kc + (size_t)c * 131072; step = 1024; voff = (lane >> 4) * 256 + ((lane & 15) ^ ((lane >> 4) << 2)) * 16; cnt = 16; }
      else if (seg == 2) { base = (const char*)p.vc + (size_t)c * 131072; step = 1024; voff = (lane >> 4) * 256 + c256; cnt = 16; }
      else if (seg == 3) {
        base = (const char*)p.wg + (size_t)(256 * ks) * 7168 + cg * 128; step = 8 * 7168; voff = (lane >> 3) * 7168 + c128; cnt = heavy ? 8 : 0;
        d1 = (const char*)p.wg - (const char*)p.wu; d0 = -d1 - 64 * 7168;
      }
      else if (seg == 4) { base = (const char*)p.wo + (size_t)(128 * (c >> 3)) * 2048 + (c & 7) * 256; step = 4 * 2048; voff = (lane >> 4) * 2048 + c256; cnt = 4; }
      else { base = (const char*)p.wd + (size_t)(64 * cg) * 2048 + ks * 512; step = 2 * 2048; voff = (lane >> 5) * 2048 + ((lane & 31) ^ ((lane >> 5) << 1)) * 16; cnt = heavy ? 4 : 0; }
#pragma unroll 1
      for (int i = 0; i < cnt; ++i) {
#pragma unroll
        for (int j = 0; j < 8; ++j) { GLDS(base + voff, L + (ldsoff + j * 1024)); base += step; }
        base += (i & 1) ? d1 : d0;
        ldsoff += 8192; if (ldsoff == RING) ldsoff = 0;
        ++n;
        if (pub >= 2 && !(pub & 1)) ST((pub >> 1) - 1);
        if (n == WIN / 8 + 1) __builtin_amdgcn_s_barrier();
        if (n >= 2 * NSLOT && !(n & 1)) {
          const int fp = (n >> 1) - NSLOT;
          if ((unsigned)__builtin_amdgcn_readfirstlane(flg[F_FREE + fp]) < 8u) {
            waitvm<0>();
            pub = (unsigned)n; flg[F_FULL] = pub;
            lds_wait_ge(flg, F_FREE + fp, 8u);
          }
        }
        if (n > WIN / 8) {
          waitvm<WIN>();
          if (pub < (unsigned)(n - WIN / 8)) { pub = (unsigned)(n - WIN / 8); flg[F_FULL] = pub; }
        }
      }
    }
    if (WIN >= 32) { waitvm<24>(); if (pub < (unsigned)(n - 3)) { pub = (unsigned)(n - 3); flg[F_FULL] = pub; } }
    if (WIN >= 24) { waitvm<16>(); if (pub < (unsigned)(n - 2)) { pub = (unsigned)(n - 2); flg[F_FULL] = pub; } }
    ST((n >> 1) - 2);
    waitvm<8>();
    if (pub < (unsigned)(n - 1)) { pub = (unsigned)(n - 1); flg[F_FULL] = pub; }
    waitvm<0>();
    flg[F_FULL] = (unsigned)n; ST((n >> 1) - 1);
    ST(31);
    STR(29);
    if (lane == 0) { ((GAS unsigned long long*)(p.ws + WS_DBG))[c * 4] = xt0; ((GAS unsigned long long*)(p.ws + WS_DBG))[c * 4 + 2] = __builtin_amdgcn_s_memrealtime(); }
    return;
  }

  const __amdgpu_buffer_rsrc_t rs = __builtin_amdgcn_make_buffer_rsrc((void*)p.ws, 0, WS_END, 0x00020000);
  const bool dry = wave >= 10;
  const int lw = dry ? 4096 : lane;
  const unsigned wm = dry ? 0u : ~0u;
  const unsigned glim = dry ? 1u : (1u << 14);

  if (wave == 1 || wave == 11) {
    const unsigned E = __hip_atomic_load((GAS unsigned*)(p.ws + WS_EPOCH), __ATOMIC_RELAXED, __HIP_MEMORY_SCOPE_AGENT);
    if (lw < 64) flg[lane] = lane == F_EPOCH ? E : 0u;
    const int fj = lane & 7;
    const unsigned xa = p.x[(2 * (fj & 1)) * 1024 + 4 * c + (fj >> 1)], xb = p.x[(2 * (fj & 1) + 1) * 1024 + 4 * c + (fj >> 1)];
    LGKM0();
    __builtin_amdgcn_s_barrier();
    const unsigned tA = __builtin_amdgcn_readfirstlane(E) + 1u, tB = tA ^ 0x9E3779B9u;
    ST(32);
    lds_wait_ge(flg, F_PUB1, 8u & wm);
    ST(33);
    {
      v4u v[4];
      gather<4>(rs, WS_QP + c * 4096 + lane * 16, 1024, WS_QP + c * 4096 + lane * 16 + 3072, v, tA, tB, glim);
      float a = 0.f, b = 0.f;
#pragma unroll
      for (int i = 0; i < 4; ++i) { a += __uint_as_float(v[i].x); b += __uint_as_float(v[i].z); }
      a += shx(a, 32); b += shx(b, 32);
      const unsigned pk = f2bf(a) | (f2bf(b) << 16);
      const unsigned p0 = __builtin_amdgcn_mov_dpp(pk, 0x00, 0xf, 0xf, true), p1 = __builtin_amdgcn_mov_dpp(pk, 0x55, 0xf, 0xf, true);
      const unsigned p2 = __builtin_amdgcn_mov_dpp(pk, 0xAA, 0xf, 0xf, true), p3 = __builtin_amdgcn_mov_dpp(pk, 0xFF, 0xf, 0xf, true);
      const int e = lane & 1;
      const unsigned d0 = e ? ((p0 >> 16) | (p1 & 0xffff0000u)) : ((p0 & 0xffffu) | (p1 << 16));
      const unsigned d1 = e ? ((p2 >> 16) | (p3 & 0xffff0000u)) : ((p2 & 0xffffu) | (p3 << 16));
      if (lw < 32 && (lane & 3) < 2) {
        const int bb = 2 * (lane >> 4) + e;
        gst(rs, WS_Q + bb * 16384 + (4 * c + ((lane & 15) >> 2)) * 16, d0, d1, tA, tB);
      }
    }
    ST(34);
    {
      v4u v[4];
      const int o = WS_Q + (c >> 6) * 16384 + ((c >> 4) & 3) * 4096 + lane * 16;
      gather<4>(rs, o, 1024, o + 3072, v, tA, tB, glim);
      if (lw < 64) {
#pragma unroll
        for (int i = 0; i < 4; ++i) *(LAS v2u*)(L + Q_OFF + (64 * i + lane) * 8) = v2u{v[i].x, v[i].z};
        LGKM0();
        flg[F_QRDY] = 1u;
      }
    }
    ST(35);
    lds_wait_ge(flg, F_PUB2, 8u & wm);
    ST(36);
    {
      v4u v[9];
      gather<9>(rs, WS_AP + c * AP_STRIDE + lane * 16, 1024, WS_AP + c * AP_STRIDE + 8192 + (lane & 15) * 16, v, tA, tB, glim);
      const float m = __uint_as_float(v[8].x), l = __uint_as_float(v[8].z);
      float M = m;
      M = fmaxf(M, shx(M, 1)); M = fmaxf(M, shx(M, 2)); M = fmaxf(M, shx(M, 4)); M = fmaxf(M, shx(M, 8));
      const float w = __builtin_amdgcn_exp2f((m - M) * 1.4426950408889634f);
      float Ls = w * l;
      Ls += shx(Ls, 1); Ls += shx(Ls, 2); Ls += shx(Ls, 4); Ls += shx(Ls, 8);
      float a = 0.f, b = 0.f;
#pragma unroll
      for (int i = 0; i < 8; ++i) {
        const float w0 = __builtin_bit_cast(float, __builtin_amdgcn_readlane(__builtin_bit_cast(int, w), 2 * i));
        const float w1 = __builtin_bit_cast(float, __builtin_amdgcn_readlane(__builtin_bit_cast(int, w), 2 * i + 1));
        const float ws_ = lane < 32 ? w0 : w1;
        a = fmaf(ws_, __uint_as_float(v[i].x), a); b = fmaf(ws_, __uint_as_float(v[i].z), b);
      }
      a += shx(a, 32); b += shx(b, 32);
      const unsigned pk = f2bf(a / Ls) | (f2bf(b / Ls) << 16);
      const unsigned nb = __builtin_amdgcn_mov_dpp(pk, 0xF5, 0xf, 0xf, true);
      const int hh = ((c >> 4) & 3) * 8 + ((c & 15) >> 1), bb = c >> 6, dh = c & 1;
      if (lw < 32 && (lane & 1) == 0) gst(rs, WS_O + hh * 2048 + bb * 512 + (dh * 16 + (lane >> 1)) * 16, pk, nb, tA, tB);
    }
    ST(37);
    {
      v4u v[2];
      const int o = WS_O + (c >> 3) * 2048 + lane * 16;
      gather<2>(rs, o, 1024, o + 1024, v, tA, tB, glim);
      if (lw < 64) {
#pragma unroll
        for (int i = 0; i < 2; ++i) *(LAS v2u*)(L + O_OFF + (64 * i + lane) * 8) = v2u{v[i].x, v[i].z};
        LGKM0();
        flg[F_ORDY] = 1u;
      }
    }
    ST(38);
    lds_wait_ge(flg, F_PUB3, (heavy ? 16u : 8u) & wm);
    ST(39);
    {
      v4u v[11];
      const int o = WS_FP + c * FP_STRIDE + lane * 16;
      gather<11>(rs, o, 1024, o + 10240, v, tA, tB, glim);
      float s0 = 0.f, s1 = 0.f, y0 = 0.f, y1 = 0.f;
#pragma unroll
      for (int i = 0; i < 4; ++i) { s0 += __uint_as_float(v[i].x); s1 += __uint_as_float(v[i].z); }
#pragma unroll
      for (int i = 4; i < 11; ++i) { y0 += __uint_as_float(v[i].x); y1 += __uint_as_float(v[i].z); }
#pragma unroll
      for (int m = 8; m <= 32; m <<= 1) { s0 += shx(s0, m); s1 += shx(s1, m); y0 += shx(y0, m); y1 += shx(y1, m); }
      s0 = rbf(s0); s1 = rbf(s1); y0 = rbf(y0); y1 = rbf(y1);
      const float g0 = rbf(y0 / (1.f + __expf(-y0))), g1 = rbf(y1 / (1.f + __expf(-y1)));
      const unsigned o0 = f2bf(bf2f(xa) + rbf(s0 * g0)), o1 = f2bf(bf2f(xb) + rbf(s1 * g1));
      if (lw < 8) {
        p.out[(2 * (fj & 1)) * 1024 + 4 * c + (fj >> 1)] = (u16)o0;
        p.out[(2 * (fj & 1) + 1) * 1024 + 4 * c + (fj >> 1)] = (u16)o1;
      }
      if (c == 0 && lw == 0) __hip_atomic_store((GAS unsigned*)(p.ws + WS_EPOCH), tA, __ATOMIC_RELAXED, __HIP_MEMORY_SCOPE_AGENT);
    }
    ST(40);
    if (dry) { STD(42); }
    if (lane == 0) ((GAS unsigned long long*)(p.ws + WS_DBG))[c * 4 + 1] = __builtin_amdgcn_s_memrealtime();
    return;
  }

  const int w = dry ? 0 : wave - 2;
  const int g = lane >> 4, q4 = (lane & 15) >> 2, p4 = lane & 3;
  v4u xv0 = {0, 0, 0, 0}, xv1 = {0, 0, 0, 0};
  if (w < 4) {
    xv0 = *(const GAS v4u*)(p.x + w * 1024 + 8 * lane);
    xv1 = *(const GAS v4u*)(p.x + w * 1024 + 512 + 8 * lane);
  }
  __builtin_amdgcn_s_barrier();
  const unsigned tA = __builtin_amdgcn_readfirstlane(flg[F_EPOCH]) + 1u, tB = tA ^ 0x9E3779B9u;
  const int sb = 48 + w * 16;
  ST(sb + 0);
  if (w < 4) {
    float xf[16];
#pragma unroll
    for (int i = 0; i < 4; ++i) {
      xf[2 * i] = bf2f(xv0[i] & 0xffffu); xf[2 * i + 1] = bf2f(xv0[i] >> 16);
      xf[8 + 2 * i] = bf2f(xv1[i] & 0xffffu); xf[8 + 2 * i + 1] = bf2f(xv1[i] >> 16);
    }
    float s = 0.f;
#pragma unroll
    for (int i = 0; i < 16; ++i) s += xf[i];
#pragma unroll
    for (int m = 1; m <= 32; m <<= 1) s += shx(s, m);
    const float mean = s * (1.f / 1024.f);
    float vv = 0.f;
#pragma unroll
    for (int i = 0; i < 16; ++i) { const float d = xf[i] - mean; vv = fmaf(d, d, vv); }
#pragma unroll
    for (int m = 1; m <= 32; m <<= 1) vv += shx(vv, m);
    const float mb = rbf(mean), vb = rbf(vv * (1.f / 1024.f));
    const float sd = rbf(sqrtf(rbf(vb + 1e-5f)));
    const int hi = ks >> 1;
    unsigned o[4];
#pragma unroll
    for (int i = 0; i < 4; ++i) {
      const float a = hi ? xf[8 + 2 * i] : xf[2 * i], b = hi ? xf[8 + 2 * i + 1] : xf[2 * i + 1];
      o[i] = f2bf(rbf(a - mb) / sd) | (f2bf(rbf(b - mb) / sd) << 16);
    }
    if ((lw >> 5) == (ks & 1)) *(LAS v4u*)(L + XN_OFF + w * 512 + (lane & 31) * 16) = v4u{o[0], o[1], o[2], o[3]};
    lds_add(flg, F_XN, 1u, lw);
  }
  lds_wait_ge(flg, F_XN, 4u & wm);
  ST(sb + 1);
  const int arow = (lane & 3);
  const int trow = 8 * g + q4;
  {
    const int ct = w & 3, kh = w >> 2;
    lds_wait_ge(flg, F_FULL, (unsigned)(2 * kh + 2) & wm);
    LAS unsigned char* tb = L + kh * SLOT + trow * 128 + 32 * (ct ^ (q4 >> 1)) + 8 * p4;
    LAS unsigned char* ab = L + XN_OFF + arow * 512 + kh * 256 + 16 * g;
    f32x4 acc = {0.f, 0.f, 0.f, 0.f};
    v4u bfr[4], afr[4];
#pragma unroll
    for (int k = 0; k < 4; ++k) { bfr[k] = trfrag(tb + k * 4096, 512); afr[k] = ldsr128(ab + k * 64); }
    lds_add(flg, F_FREE + kh, 2u, lw);
#pragma unroll
    for (int k = 0; k < 4; ++k) acc = mfma(afr[k], bfr[k], acc);
    if (lw < 32) {
      const int bp = lane >> 4;
      gst(rs, WS_QP + (4 * cg + ct) * 4096 + (2 * ks + kh) * 512 + lane * 16, __float_as_uint(bp ? acc[2] : acc[0]), __float_as_uint(bp ? acc[3] : acc[1]), tA, tB);
    }
    lds_add(flg, F_PUB1, 1u, lw);
  }
  ST(sb + 2);
  float sc[16];
  {
    lds_wait_ge(flg, F_FULL, (unsigned)(2 * (2 + w + 1)) & wm);
    const int i = lane & 15;
    const int slot = (2 + w) % NSLOT;
    LAS unsigned char* kb = L + slot * SLOT + (8 * (i >> 2) + (i & 3)) * 256 + 16 * g;
    v4u kf[16];
#pragma unroll
    for (int kt = 0; kt < 4; ++kt)
#pragma unroll
      for (int s = 0; s < 4; ++s) kf[kt * 4 + s] = ldsr128(kb + ((kt >> 1) * 32 + (kt & 1) * 4) * 256 + 64 * (s ^ (i & 3)));
    lds_add(flg, F_FREE + 2 + w, 8u, lw);
    lds_wait_ge(flg, F_QRDY, 1u & wm);
    ST(sb + 3);
    v4u qf[4];
#pragma unroll
    for (int s = 0; s < 4; ++s) qf[s] = ldsr128(L + Q_OFF + (lane & 7) * 256 + 64 * s + 16 * g);
    float mx = -INFINITY;
#pragma unroll
    for (int kt = 0; kt < 4; ++kt) {
      f32x4 acc = {0.f, 0.f, 0.f, 0.f};
#pragma unroll
      for (int s = 0; s < 4; ++s) acc = mfma(kf[kt * 4 + s], qf[s], acc);
#pragma unroll
      for (int r = 0; r < 4; ++r) { const float v = rbf(rbf(acc[r]) * 0.08838834764831845f); sc[kt * 4 + r] = v; mx = fmaxf(mx, v); }
    }
    mx = fmaxf(mx, shx(mx, 16)); mx = fmaxf(mx, shx(mx, 32));
    if (lw < 8) ((LAS float*)(L + MAX_OFF))[w * 8 + lane] = mx;
    lds_add(flg, F_MAX, 1u, lw);
  }
  ST(sb + 4);
  float mcu;
  {
    lds_wait_ge(flg, F_MAX, 8u & wm);
    mcu = -INFINITY;
#pragma unroll
    for (int ww = 0; ww < 8; ++ww) mcu = fmaxf(mcu, ((LAS float*)(L + MAX_OFF))[ww * 8 + (lane & 7)]);
    float sum = 0.f;
    unsigned pk[8];
#pragma unroll
    for (int i = 0; i < 8; ++i) {
      const float a = __builtin_amdgcn_exp2f((sc[2 * i] - mcu) * 1.4426950408889634f), b = __builtin_amdgcn_exp2f((sc[2 * i + 1] - mcu) * 1.4426950408889634f);
      sum += a + b;
      pk[i] = f2bf(a) | (f2bf(b) << 16);
    }
    sum += shx(sum, 16); sum += shx(sum, 32);
    if (lw < 8) ((LAS float*)(L + SUM_OFF))[w * 8 + lane] = sum;
    if ((lw & (4096 + 8)) == 0) {
      *(LAS v4u*)(L + P_OFF + (2 * w) * 512 + g * 128 + (lane & 7) * 16) = v4u{pk[0], pk[1], pk[2], pk[3]};
      *(LAS v4u*)(L + P_OFF + (2 * w + 1) * 512 + g * 128 + (lane & 7) * 16) = v4u{pk[4], pk[5], pk[6], pk[7]};
    }
    lds_add(flg, F_P, 1u, lw);
  }
  ST(sb + 5);
  {
    lds_wait_ge(flg, F_P, 8u & wm);
    f32x4 acc = {0.f, 0.f, 0.f, 0.f};
    const int voff = trow * 256 + 32 * (w ^ q4) + 8 * p4;
    LAS unsigned char* pb = L + P_OFF + g * 128 + (lane & 7) * 16;
    int slot = (10) % NSLOT;
#pragma unroll 1
    for (int fi = 0; fi < 8; ++fi) {
      const v4u pf0 = ldsr128(pb + (2 * fi) * 512), pf1 = ldsr128(pb + (2 * fi + 1) * 512);
      lds_wait_ge(flg, F_FULL, (unsigned)(2 * (10 + fi + 1)) & wm);
      LAS unsigned char* vb = L + slot * SLOT + voff;
      const v4u a0 = trfrag(vb, 1024), a1 = trfrag(vb + 8192, 1024);
      lds_add(flg, F_FREE + 10 + fi, 1u, lw);
      acc = mfma(a0, pf0, acc);
      acc = mfma(a1, pf1, acc);
      slot = (slot + 1 == NSLOT) ? 0 : slot + 1;
    }
    ST(sb + 6);
    const int hd = lane & 7, half = (lane >> 3) & 1;
    const int grp = c >> 4, s = c & 15;
    if (lw < 64) gst(rs, WS_AP + (grp * 16 + hd * 2 + (w >> 2)) * AP_STRIDE + s * 512 + ((w & 3) * 8 + 2 * g + half) * 16,
        __float_as_uint(half ? acc[2] : acc[0]), __float_as_uint(half ? acc[3] : acc[1]), tA, tB);
    if (w == 0) {
      float lsum = 0.f;
#pragma unroll
      for (int ww = 0; ww < 8; ++ww) lsum += ((LAS float*)(L + SUM_OFF))[ww * 8 + hd];
      if (lw < 16) gst(rs, WS_AP + (grp * 16 + hd * 2 + half) * AP_STRIDE + 8192 + s * 16, __float_as_uint(mcu), __float_as_uint(lsum), tA, tB);
    }
    lds_add(flg, F_PUB2, 1u, lw);
  }
  ST(sb + 7);
  int f = 18;
  if (heavy) {
    const int mat = w >> 2, ct = w & 3;
    f32x4 acc = {0.f, 0.f, 0.f, 0.f};
    const int toff = mat * 8192 + trow * 128 + 32 * (ct ^ (q4 >> 1)) + 8 * p4;
    LAS unsigned char* ab = L + XN_OFF + arow * 512 + 16 * g;
#pragma unroll 1
    for (int fi = 0; fi < 4; ++fi) {
      const v4u a0 = ldsr128(ab + fi * 128), a1 = ldsr128(ab + fi * 128 + 64);
      lds_wait_ge(flg, F_FULL, (unsigned)(2 * (18 + fi) + 1 + mat) & wm);
      LAS unsigned char* tb = L + fi * SLOT + toff;
      const v4u b0 = trfrag(tb, 512), b1 = trfrag(tb + 4096, 512);
      acc = mfma(a0, b0, acc);
      acc = mfma(a1, b1, acc);
    }
    if (lw < 32) {
      const int bp = lane >> 4;
      gst(rs, WS_GP + cg * 16384 + ks * 4096 + w * 512 + lane * 16, __float_as_uint(bp ? acc[2] : acc[0]), __float_as_uint(bp ? acc[3] : acc[1]), tA, tB);
    }
    f = 22;
    ST(sb + 8);
    {
      v4u v[2];
      const int ct2 = w >> 1, n3 = lane & 7, bp = (lane >> 3) & 1;
      const int o = WS_GP + cg * 16384 + g * 4096 + ct2 * 512 + (bp * 16 + (w & 1) * 8 + n3) * 16;
      gather<2>(rs, o, 2048, o + 2048, v, tA, tB, glim);
      float g0 = __uint_as_float(v[0].x), g1 = __uint_as_float(v[0].z), u0 = __uint_as_float(v[1].x), u1 = __uint_as_float(v[1].z);
      g0 += shx(g0, 16); g1 += shx(g1, 16); u0 += shx(u0, 16); u1 += shx(u1, 16);
      g0 += shx(g0, 32); g1 += shx(g1, 32); u0 += shx(u0, 32); u1 += shx(u1, 32);
      g0 = rbf(g0); g1 = rbf(g1); u0 = rbf(u0); u1 = rbf(u1);
      const float z0 = 0.7978845608028654f * (g0 + 0.044715f * g0 * g0 * g0), z1 = 0.7978845608028654f * (g1 + 0.044715f * g1 * g1 * g1);
      const float t0 = 1.f - 2.f / (__expf(2.f * z0) + 1.f), t1 = 1.f - 2.f / (__expf(2.f * z1) + 1.f);
      const unsigned h0 = f2bf(rbf(0.5f * g0 * (1.f + t0)) * u0), h1 = f2bf(rbf(0.5f * g1 * (1.f + t1)) * u1);
      if (lw < 16) {
        const int fcol = 16 * ct2 + 8 * (w & 1) + n3;
        ((LAS u16*)(L + H_OFF))[(2 * bp) * 64 + fcol] = (u16)h0;
        ((LAS u16*)(L + H_OFF))[(2 * bp + 1) * 64 + fcol] = (u16)h1;
      }
      lds_add(flg, F_H, 1u, lw);
    }
    ST(sb + 9);
  }
  {
    lds_wait_ge(flg, F_ORDY, 1u & wm);
    ST(sb + 10);
    f32x4 acc = {0.f, 0.f, 0.f, 0.f};
    const int toff = trow * 256 + 32 * (w ^ q4) + 8 * p4;
    LAS unsigned char* ab = L + O_OFF + arow * 256 + 16 * g;
#pragma unroll 1
    for (int fi = 0; fi < 2; ++fi) {
      const v4u a0 = ldsr128(ab + fi * 128), a1 = ldsr128(ab + fi * 128 + 64);
      lds_wait_ge(flg, F_FULL, (unsigned)(2 * (f + fi + 1)) & wm);
      LAS unsigned char* tb = L + (f - 18 + fi) * SLOT + toff;
      const v4u b0 = trfrag(tb, 1024), b1 = trfrag(tb + 8192, 1024);
      acc = mfma(a0, b0, acc);
      acc = mfma(a1, b1, acc);
    }
    if (lw < 32) {
      const int bp = lane >> 4, n = lane & 15;
      gst(rs, WS_FP + (32 * (c & 7) + 4 * w + (n >> 2)) * FP_STRIDE + (c >> 3) * 128 + ((n & 3) * 2 + bp) * 16,
          __float_as_uint(bp ? acc[2] : acc[0]), __float_as_uint(bp ? acc[3] : acc[1]), tA, tB);
    }
    lds_add(flg, F_PUB3, 1u, lw);
  }
  ST(sb + 11);
  if (heavy) {
    lds_wait_ge(flg, F_H, 8u & wm);
    f32x4 acc0 = {0.f, 0.f, 0.f, 0.f}, acc1 = {0.f, 0.f, 0.f, 0.f};
    const int toff0 = trow * 512 + 32 * ((2 * w) ^ (q4 & 1)) + 8 * p4, toff1 = trow * 512 + 32 * ((2 * w + 1) ^ (q4 & 1)) + 8 * p4;
    LAS unsigned char* ab = L + H_OFF + arow * 128 + 16 * g;
#pragma unroll 1
    for (int fi = 0; fi < 2; ++fi) {
      const v4u a0 = ldsr128(ab + fi * 64);
      lds_wait_ge(flg, F_FULL, (unsigned)(2 * (24 + fi + 1)) & wm);
      LAS unsigned char* tb = L + (6 + fi) * SLOT;
      const v4u b0 = trfrag(tb + toff0, 2048), b1 = trfrag(tb + toff1, 2048);
      acc0 = mfma(a0, b0, acc0);
      acc1 = mfma(a0, b1, acc1);
    }
    const int bp = g & 1, tl = g >> 1, n = lane & 15;
    const float e0 = tl ? (bp ? acc1[2] : acc1[0]) : (bp ? acc0[2] : acc0[0]);
    const float e1 = tl ? (bp ? acc1[3] : acc1[1]) : (bp ? acc0[3] : acc0[1]);
    if (lw < 64) gst(rs, WS_FP + (64 * ks + 8 * w + 4 * tl + (n >> 2)) * FP_STRIDE + (32 + cg) * 128 + ((n & 3) * 2 + bp) * 16, __float_as_uint(e0), __float_as_uint(e1), tA, tB);
    lds_add(flg, F_PUB3, 1u, lw);
  }
  ST(sb + 12);
  if (dry) { STD(41); }
  if (!dry) DUMP(sb, 16);
}

}

namespace fk {
__global__ void __launch_bounds__(64) spin_kernel(Params p, int K, int which) {
  const int lane = threadIdx.x;
  unsigned long long mn = ~0ull, mx = 0;
  for (int c = lane; c < 256; c += 64) {
    const unsigned long long a = ((const GAS unsigned long long*)(p.ws + WS_DBG))[c * 4], b = ((const GAS unsigned long long*)(p.ws + WS_DBG))[c * 4 + which];
    mn = a < mn ? a : mn; mx = b > mx ? b : mx;
  }
  for (int m = 1; m < 64; m <<= 1) { const unsigned long long a = __shfl_xor(mn, m, 64), b = __shfl_xor(mx, m, 64); mn = a < mn ? a : mn; mx = b > mx ? b : mx; }
  unsigned long long span = mx - mn; if (span > 10000) span = 10000;
  const unsigned long long t0 = __builtin_amdgcn_s_memrealtime();
  while (__builtin_amdgcn_s_memrealtime() - t0 < span * K) __builtin_amdgcn_s_sleep(4);
}
}

extern "C" void kernel_launch(void* const* d_in, const int* in_sizes, int n_in, void* d_out, int out_size, void* d_ws, size_t ws_size, hipStream_t stream) {
  fk::Params p;
  p.x = (const u16*)d_in[0]; p.kc = (const u16*)d_in[1]; p.vc = (const u16*)d_in[2]; p.wq = (const u16*)d_in[3]; p.wo = (const u16*)d_in[4];
  p.wg = (const u16*)d_in[6]; p.wu = (const u16*)d_in[7]; p.wd = (const u16*)d_in[8];
  p.out = (u16*)d_out; p.ws = (unsigned char*)d_ws;
  (void)hipFuncSetAttribute((const void*)fk::fused_kernel, hipFuncAttributeMaxDynamicSharedMemorySize, fk::LDS_BYTES);
  hipLaunchKernelGGL(fk::fused_kernel, dim3(fk::NCU), dim3(64 * fk::NWAVES), fk::LDS_BYTES, stream, p);
  hipLaunchKernelGGL(fk::spin_kernel, dim3(1), dim3(64), 0, stream, p, 0, 1);
}
```

```cpp
#include <hip/hip_runtime.h>
#include <stdint.h>

#define GAS __attribute__((address_space(1)))
#define LAS __attribute__((address_space(3)))
typedef unsigned short u16;
typedef unsigned v4u __attribute__((ext_vector_type(4)));
typedef unsigned v2u __attribute__((ext_vector_type(2)));
typedef float f32x4 __attribute__((ext_vector_type(4)));
typedef float f32x2 __attribute__((ext_vector_type(2)));
typedef short s16x4 __attribute__((ext_vector_type(4)));
typedef __bf16 bf16x8 __attribute__((ext_vector_type(8)));
typedef __bf16 bf16x2 __attribute__((ext_vector_type(2)));

namespace fk {

constexpr int NCU = 256, NWAVES = 10;
constexpr int NQ = 34;
constexpr int RING = NQ * 4096;
constexpr int XN_OFF = RING;
constexpr int O_OFF = XN_OFF + 2048;
constexpr int Q_OFF = O_OFF + 2048;
constexpr int H_OFF = Q_OFF + 2048;
constexpr int P_OFF = H_OFF + 2048;
constexpr int MU_OFF = P_OFF + 20 * 512, LU_OFF = MU_OFF + 768, FLG_OFF = LU_OFF + 768;
constexpr int LDS_BYTES = 163840;
static_assert(FLG_OFF + 256 <= LDS_BYTES, "lds");
constexpr int F_FULL = 0, F_QRDY = 1, F_PROG = 8, F_STAGE = 16;
constexpr int WS_EPOCH = 0;
constexpr int WS_QP = 4096;
constexpr int WS_Q = WS_QP + 256 * 2048;
constexpr int AP_STRIDE = 16 * 640;
constexpr int WS_AP = WS_Q + 65536;
constexpr int WS_O = WS_AP + 128 * AP_STRIDE;
constexpr int WS_GP = WS_O + 65536;
constexpr int WS_H = WS_GP + 56 * 16384;
constexpr int WS_FP = WS_H + 65536;
constexpr int WS_END = WS_FP + 256 * 4096;
constexpr int WIN = 20;
constexpr int FDQ = 16, FDQ2 = 40, BOQ = 16;
constexpr int FD1 = 12, BO1 = 8, FD2 = 4, FD3 = 18, BO2 = 4, LEADR = 2;

struct Params {
  const u16* x; const u16* kc; const u16* vc; const u16* wq; const u16* wo; const u16* wg; const u16* wu; const u16* wd;
  u16* out; unsigned char* ws;
};

__device__ __forceinline__ float bflo(unsigned u) { return __uint_as_float(u << 16); }
__device__ __forceinline__ float bfhi(unsigned u) { return __uint_as_float(u & 0xffff0000u); }
__device__ __forceinline__ unsigned pk2(float a, float b) { return __builtin_bit_cast(unsigned, __builtin_convertvector(f32x2{a, b}, bf16x2)); }
__device__ __forceinline__ float rbf(float f) { return bflo(pk2(f, f)); }
__device__ __forceinline__ f32x4 mfma(v4u a, v4u b, f32x4 c) {
  return __builtin_amdgcn_mfma_f32_16x16x32_bf16(__builtin_bit_cast(bf16x8, a), __builtin_bit_cast(bf16x8, b), c, 0, 0, 0);
}
__device__ __forceinline__ v2u trread(LAS unsigned char* p) {
  return __builtin_bit_cast(v2u, __builtin_amdgcn_ds_read_tr16_b64_v4i16((LAS s16x4*)p));
}
__device__ __forceinline__ v4u trfrag(LAS unsigned char* p, int off_t1) {
  const v2u a = trread(p), b = trread(p + off_t1);
  return v4u{a.x, a.y, b.x, b.y};
}
__device__ __forceinline__ v4u ldsr128(LAS unsigned char* p) { return *(LAS v4u*)p; }
#define LGKM0() asm volatile("s_waitcnt lgkmcnt(0)" ::: "memory")
#define CBAR() asm volatile("" ::: "memory")
template <int N> __device__ __forceinline__ void waitvm() { asm volatile("s_waitcnt vmcnt(%0)" ::"n"(N) : "memory"); }
#define GLDS(src, dst) __builtin_amdgcn_global_load_lds((const unsigned*)(src), (LAS unsigned*)(dst), 16, 0, 2)

template <int CTRL> __device__ __forceinline__ float dpp(float x) {
  return __builtin_bit_cast(float, __builtin_amdgcn_mov_dpp(__builtin_bit_cast(int, x), CTRL, 0xf, 0xf, true));
}
__device__ __forceinline__ float sum16(float x) { x += dpp<0x128>(x); x += dpp<0x124>(x); x += dpp<0x122>(x); x += dpp<0x121>(x); return x; }
__device__ __forceinline__ float sumx16(float x) { auto s = __builtin_amdgcn_permlane16_swap(__float_as_uint(x), __float_as_uint(x), false, false); return __uint_as_float(s[0]) + __uint_as_float(s[1]); }
__device__ __forceinline__ float sumx32(float x) { auto s = __builtin_amdgcn_permlane32_swap(__float_as_uint(x), __float_as_uint(x), false, false); return __uint_as_float(s[0]) + __uint_as_float(s[1]); }
__device__ __forceinline__ float maxx16(float x) { auto s = __builtin_amdgcn_permlane16_swap(__float_as_uint(x), __float_as_uint(x), false, false); return fmaxf(__uint_as_float(s[0]), __uint_as_float(s[1])); }
__device__ __forceinline__ float maxx32(float x) { auto s = __builtin_amdgcn_permlane32_swap(__float_as_uint(x), __float_as_uint(x), false, false); return fmaxf(__uint_as_float(s[0]), __uint_as_float(s[1])); }
__device__ __forceinline__ float sumrows(float x) { return sumx32(sumx16(x)); }
__device__ __forceinline__ float maxrows(float x) { return maxx32(maxx16(x)); }
__device__ __forceinline__ float sum8rows(float x) { return sumrows(x + dpp<0x128>(x)); }
__device__ __forceinline__ float max8rows(float x) { return maxrows(fmaxf(x, dpp<0x128>(x))); }
__device__ __forceinline__ float frcp(float x) { return __builtin_amdgcn_rcpf(x); }
__device__ __forceinline__ float fexp(float x) { return __builtin_amdgcn_exp2f(x * 1.4426950408889634f); }

__device__ __forceinline__ void lds_wait(volatile LAS unsigned* word, unsigned want) {
#pragma unroll 1
  for (unsigned it = 1u << 17; it; --it) {
    if (__builtin_amdgcn_ballot_w64(*word < want) == 0ull) break;
    __builtin_amdgcn_s_sleep(1);
  }
  CBAR();
}
__device__ __forceinline__ void lds_spin(volatile LAS unsigned* word, unsigned want) {
#pragma unroll 1
  for (unsigned it = 1u << 18; it; --it) if (__builtin_amdgcn_ballot_w64(*word < want) == 0ull) break;
  CBAR();
}
__device__ __forceinline__ void lds_set(volatile LAS unsigned* word, unsigned v) { LGKM0(); *word = v; CBAR(); }
__device__ __forceinline__ void gst(__amdgpu_buffer_rsrc_t rs, int off, unsigned a, unsigned b, unsigned tA, unsigned tB) {
  __builtin_amdgcn_raw_buffer_store_b128(v4u{a, tA, b, tB}, rs, off, 0, 16);
}
__device__ __forceinline__ v4u gld(__amdgpu_buffer_rsrc_t rs, int off) { return __builtin_amdgcn_raw_buffer_load_b128(rs, off, 0, 16); }
template <int N, int BO, int FD>
__device__ __forceinline__ void poll(__amdgpu_buffer_rsrc_t rs, const int (&off)[N], v4u (&v)[N], unsigned tA, unsigned tB) {
  if (FD) __builtin_amdgcn_s_sleep(FD);
  CBAR();
#pragma unroll
  for (int k = 0; k < N; ++k) v[k] = gld(rs, off[k]);
#pragma unroll 1
  for (unsigned it = 1u << 15; it; --it) {
    bool bad[N]; bool any = false;
#pragma unroll
    for (int k = 0; k < N; ++k) { bad[k] = (v[k].y != tA) | (v[k].w != tB); any |= bad[k]; }
    if (__builtin_amdgcn_ballot_w64(any) == 0ull) break;
    if (BO) __builtin_amdgcn_s_sleep(BO);
    CBAR();
#pragma unroll
    for (int k = 0; k < N; ++k) if (bad[k]) v[k] = gld(rs, off[k]);
  }
}
__device__ __forceinline__ void quad_pack(unsigned pk, int lane, unsigned& d0, unsigned& d1) {
  const unsigned p0 = __builtin_amdgcn_mov_dpp(pk, 0x00, 0xf, 0xf, true), p1 = __builtin_amdgcn_mov_dpp(pk, 0x55, 0xf, 0xf, true);
  const unsigned p2 = __builtin_amdgcn_mov_dpp(pk, 0xAA, 0xf, 0xf, true), p3 = __builtin_amdgcn_mov_dpp(pk, 0xFF, 0xf, 0xf, true);
  const bool e = (lane & 1) != 0;
  d0 = e ? ((p0 >> 16) | (p1 & 0xffff0000u)) : ((p0 & 0xffffu) | (p1 << 16));
  d1 = e ? ((p2 >> 16) | (p3 & 0xffff0000u)) : ((p2 & 0xffffu) | (p3 << 16));
}

__global__ void __launch_bounds__(64 * NWAVES) fused_kernel(Params p) {
  extern __shared__ __attribute__((aligned(16))) unsigned char smem[];
  LAS unsigned char* L = (LAS unsigned char*)smem;
  volatile LAS unsigned* flg = (volatile LAS unsigned*)(L + FLG_OFF);
  const int tid = threadIdx.x, lane = tid & 63, wave = __builtin_amdgcn_readfirstlane(tid >> 6);
  const int c = blockIdx.x;
  const int ks = c & 3, cg = c >> 2;
  const int grp = c >> 4, sp = c & 15, kvh = c >> 6, bat = grp & 3;
  const int lt0 = (grp & 1) ? 12 : 14;
  const bool heavy = (sp >> 1) != (lt0 >> 1);
  const int j8 = (c & 31) >> 2;
  const int mc = (c >> 5) * 7 + ((ks >= 2 && j8 > 3) ? j8 - 1 : j8);
  const int mc2 = mc >= 28 ? mc - 28 : mc + 28;
  const int spd = sp ^ 4;
  const int hidx = sp < lt0 ? sp : sp - 2;
  const int u0 = heavy ? 15 * hidx + (hidx < 6 ? hidx : 6) : 216 + 20 * (sp - lt0);
  const int NU = heavy ? (hidx < 6 ? 16 : 15) : 20;
  const int gK = heavy ? 24 : 8;

  if (wave == 0) {
    __builtin_amdgcn_s_setprio(3);
    const unsigned c128 = ((lane & 7) ^ (((lane >> 4) & 1) << 1)) * 16;
    {
      const char* base = (const char*)p.wq + (size_t)(256 * ks) * 8192 + cg * 128 + ((lane >> 3) * 8192 + c128);
#pragma unroll
      for (int j = 0; j < 32; ++j) GLDS(base + (size_t)j * (8 * 8192), L + j * 1024);
    }
    __builtin_amdgcn_s_barrier();
    unsigned n = 8;
    unsigned ldsoff = 8 * 4096, pub = 4, limit = NQ;
    const size_t kvoff = ((size_t)(bat * 4 + kvh) * 256 + u0) * 8192;
    waitvm<16>(); flg[F_FULL] = pub;
#pragma unroll 1
    for (int seg = 1; seg < 5; ++seg) {
      const char* base; unsigned step, voff; int cnt; long long d0 = 0, d1 = 0;
      if (seg == 4) {
        base = (const char*)p.wo + (size_t)(256 * grp) * 2048 + sp * 128; step = 8 * 2048; voff = (lane >> 3) * 2048 + c128; cnt = 15;
        d1 = base - ((const char*)p.wd + (size_t)(224 * grp) * 2048 + spd * 128); d0 = -d1 - 32 * 2048;
      } else if (seg == 1) {
        base = (const char*)p.wg + (size_t)(256 * ks) * 7168 + mc * 128; step = 8 * 7168; voff = (lane >> 3) * 7168 + c128; cnt = heavy ? 16 : 0;
        d1 = ((const char*)p.wg + mc * 128) - ((const char*)p.wu + mc2 * 128); d0 = -d1 - 32 * 7168;
      }
      else if (seg == 2) { base = (const char*)p.kc + kvoff; step = 1024; voff = (lane >> 4) * 256 + ((lane & 15) ^ ((lane >> 4) << 2)) * 16; cnt = 2 * NU; }
      else { base = (const char*)p.vc + kvoff; step = 1024; voff = (lane >> 4) * 256 + ((lane & 15) ^ ((lane >> 4) << 1)) * 16; cnt = 2 * NU; }
#pragma unroll 1
      for (int i = 0; i < cnt; ++i) {
        if (__builtin_expect(n >= limit, 0)) {
#pragma unroll 1
          for (unsigned it = 1u << 17; it; --it) {
            unsigned pv = flg[F_PROG + (lane & 7)];
            pv = min(pv, (unsigned)__builtin_amdgcn_mov_dpp(pv, 0x124, 0xf, 0xf, true));
            pv = min(pv, (unsigned)__builtin_amdgcn_mov_dpp(pv, 0x122, 0xf, 0xf, true));
            pv = min(pv, (unsigned)__builtin_amdgcn_mov_dpp(pv, 0x121, 0xf, 0xf, true));
            limit = (unsigned)__builtin_amdgcn_readfirstlane(pv) + NQ;
            if (n < limit) break;
            waitvm<0>();
            pub = n; flg[F_FULL] = pub;
            __builtin_amdgcn_s_sleep(1);
          }
        }
#pragma unroll
        for (int j = 0; j < 4; ++j) { GLDS(base + voff, L + (ldsoff + j * 1024)); base += step; }
        base += (i & 1) ? d1 : d0;
        ldsoff = (ldsoff + 4096 == RING) ? 0u : ldsoff + 4096;
        ++n;
        waitvm<WIN>();
        pub = max(pub, n - WIN / 4);
        flg[F_FULL] = pub;
      }
    }
    waitvm<8>();
    pub = max(pub, n - 2); flg[F_FULL] = pub;
    waitvm<0>();
    flg[F_FULL] = n;
    return;
  }

  const __amdgpu_buffer_rsrc_t rs = __builtin_amdgcn_make_buffer_rsrc((void*)p.ws, 0, WS_END, 0x00020000);
  const unsigned E = __hip_atomic_load((GAS unsigned*)(p.ws + WS_EPOCH), __ATOMIC_RELAXED, __HIP_MEMORY_SCOPE_AGENT);
  volatile LAS unsigned* stage8 = &flg[F_STAGE + (lane & 7)];
  const int g = lane >> 4;

  if (wave == 1) {
    flg[lane] = 0u;
    const int oidx = (2 * (c & 1)) * 1024 + 8 * (c >> 1) + (lane & 7);
    const unsigned xa = p.x[oidx], xb = p.x[oidx + 1024];
    LGKM0();
    __builtin_amdgcn_s_barrier();
    const unsigned tA = __builtin_amdgcn_readfirstlane(E) + 1u, tB = tA ^ 0x9E3779B9u;
    lds_wait(&flg[F_STAGE + (lane & 3)], 2u);
    {
      v4u v[2];
      const int off[2] = {WS_QP + c * 2048 + lane * 16, WS_QP + c * 2048 + 1024 + lane * 16};
      poll<2, BOQ, FDQ>(rs, off, v, tA, tB);
      const float a = sumx32(__uint_as_float(v[0].x) + __uint_as_float(v[1].x)), b = sumx32(__uint_as_float(v[0].z) + __uint_as_float(v[1].z));
      unsigned d0, d1;
      quad_pack(pk2(a, b), lane, d0, d1);
      const int src = ((lane >> 3) & 1) * 16 + (lane & 3) * 4 + ((lane >> 2) & 1);
      d0 = __shfl(d0, src, 64); d1 = __shfl(d1, src, 64);
      if (lane < 16) gst(rs, WS_Q + c * 256 + lane * 16, d0, d1, tA, tB);
    }
    {
      v4u v[4];
      const int o = WS_Q + (kvh * 64 + (lane >> 2)) * 256 + bat * 64 + (lane & 3) * 16;
      const int off[4] = {o, o + 4096, o + 8192, o + 12288};
      poll<4, BOQ, FDQ2>(rs, off, v, tA, tB);
#pragma unroll
      for (int i = 0; i < 4; ++i) *(LAS v2u*)(L + Q_OFF + (64 * i + lane) * 8) = v2u{v[i].x, v[i].z};
      lds_set(&flg[F_QRDY], 1u);
    }
    lds_wait(stage8, 8u);
    {
      v4u v[4];
      const int o = WS_FP + c * 4096 + lane * 16;
      const int off[4] = {o, o + 1024, o + 2048, o + 3072};
      poll<4, BO2, FD3>(rs, off, v, tA, tB);
      float a = (__uint_as_float(v[0].x) + __uint_as_float(v[1].x)) + (__uint_as_float(v[2].x) + __uint_as_float(v[3].x));
      float b = (__uint_as_float(v[0].z) + __uint_as_float(v[1].z)) + (__uint_as_float(v[2].z) + __uint_as_float(v[3].z));
      a = sumrows(a); b = sumrows(b);
      const unsigned spk = pk2(a, b);
      const unsigned yp = __builtin_amdgcn_mov_dpp(spk, 0x128, 0xf, 0xf, true);
      const float y0 = bflo(yp), y1 = bfhi(yp);
      const unsigned gp = pk2(y0 * frcp(1.f + fexp(-y0)), y1 * frcp(1.f + fexp(-y1)));
      const unsigned mp = pk2(bflo(spk) * bflo(gp), bfhi(spk) * bfhi(gp));
      const unsigned op = pk2(bflo(xa) + bflo(mp), bflo(xb) + bfhi(mp));
      if (lane < 8) {
        p.out[oidx] = (u16)op;
        p.out[oidx + 1024] = (u16)(op >> 16);
      }
      if (c == 0 && lane == 0) __hip_atomic_store((GAS unsigned*)(p.ws + WS_EPOCH), tA, __ATOMIC_RELAXED, __HIP_MEMORY_SCOPE_AGENT);
    }
    return;
  }

  const int w = wave - 2;
  const int q4 = (lane & 15) >> 2, p4 = lane & 3;
  volatile LAS unsigned* myprog = &flg[F_PROG + w];
  volatile LAS unsigned* mystage = &flg[F_STAGE + w];
  volatile LAS unsigned* full = &flg[F_FULL];
  v4u xv0 = {0, 0, 0, 0}, xv1 = {0, 0, 0, 0};
  if (w < 4) {
    xv0 = *(const GAS v4u*)(p.x + w * 1024 + 8 * lane);
    xv1 = *(const GAS v4u*)(p.x + w * 1024 + 512 + 8 * lane);
  }
  __builtin_amdgcn_s_barrier();
  if (w < 4) {
    float xf[16];
#pragma unroll
    for (int i = 0; i < 4; ++i) { xf[2 * i] = bflo(xv0[i]); xf[2 * i + 1] = bfhi(xv0[i]); xf[8 + 2 * i] = bflo(xv1[i]); xf[8 + 2 * i + 1] = bfhi(xv1[i]); }
    float s = 0.f;
#pragma unroll
    for (int i = 0; i < 16; ++i) s += xf[i];
    const float mean = sumrows(sum16(s)) * (1.f / 1024.f);
    float vv = 0.f;
#pragma unroll
    for (int i = 0; i < 16; ++i) { const float d = xf[i] - mean; vv = fmaf(d, d, vv); }
    vv = sumrows(sum16(vv));
    const float mb = rbf(mean), vb = rbf(vv * (1.f / 1024.f));
    const float rsd = frcp(rbf(__builtin_amdgcn_sqrtf(rbf(vb + 1e-5f))));
    const v4u xs = (ks >> 1) ? xv1 : xv0;
    unsigned o[4];
#pragma unroll
    for (int i = 0; i < 4; ++i) { const unsigned dd = pk2(bflo(xs[i]) - mb, bfhi(xs[i]) - mb); o[i] = pk2(bflo(dd) * rsd, bfhi(dd) * rsd); }
    if ((lane >> 5) == (ks & 1)) *(LAS v4u*)(L + XN_OFF + w * 512 + (lane & 31) * 16) = v4u{o[0], o[1], o[2], o[3]};
    lds_set(mystage, 1u);
  }
  const unsigned tA = __builtin_amdgcn_readfirstlane(E) + 1u, tB = tA ^ 0x9E3779B9u;
  const int arow = (lane & 3);
  const int trow = 8 * g + q4;
  const bool bp1 = (lane & 16) != 0;
  const bool hi8 = (lane & 8) != 0;
  const int ct = w & 3, hd = lane & 7;
  const int t128 = trow * 128 + 32 * (ct ^ (q4 >> 1)) + 8 * p4;
  {
    v4u xfr[8];
    if (w < 4) {
      f32x4 acc = {0.f, 0.f, 0.f, 0.f};
      v4u bfr[8];
      lds_wait(full, 4u);
#pragma unroll
      for (int k = 0; k < 4; ++k) bfr[k] = trfrag(L + k * 4096 + t128, 512);
      lds_wait(&flg[F_STAGE + (lane & 3)], 1u);
#pragma unroll
      for (int k = 0; k < 8; ++k) xfr[k] = ldsr128(L + XN_OFF + arow * 512 + 16 * g + k * 64);
#pragma unroll
      for (int k = 0; k < 4; ++k) acc = mfma(xfr[k], bfr[k], acc);
      lds_wait(full, 8u);
#pragma unroll
      for (int k = 4; k < 8; ++k) bfr[k] = trfrag(L + k * 4096 + t128, 512);
#pragma unroll
      for (int k = 4; k < 8; ++k) acc = mfma(xfr[k], bfr[k], acc);
      if (lane < 32) gst(rs, WS_QP + (4 * cg + w) * 2048 + ks * 512 + lane * 16, __float_as_uint(bp1 ? acc[2] : acc[0]), __float_as_uint(bp1 ? acc[3] : acc[1]), tA, tB);
      lds_set(mystage, 2u);
    } else {
      lds_wait(&flg[F_STAGE + (lane & 3)], 1u);
#pragma unroll
      for (int k = 0; k < 8; ++k) xfr[k] = ldsr128(L + XN_OFF + arow * 512 + 16 * g + k * 64);
    }
    lds_set(myprog, 8u);
    if (heavy) {
      const int mat = w >> 2;
      f32x4 gacc = {0.f, 0.f, 0.f, 0.f};
#pragma unroll
      for (int k = 0; k < 8; ++k) {
        lds_wait(full, (unsigned)(8 + 2 * k + 1 + mat));
        gacc = mfma(xfr[k], trfrag(L + (8 + 2 * k + mat) * 4096 + t128, 512), gacc);
      }
      const int gpoff = WS_GP + (w < 4 ? mc : mc2) * 16384 + ct * 4096 + (hi8 ? 2048 : 0) + (bp1 ? 1024 : 0) + ks * 256 + mat * 128 + hd * 16;
      if (lane < 32) gst(rs, gpoff, __float_as_uint(bp1 ? gacc[2] : gacc[0]), __float_as_uint(bp1 ? gacc[3] : gacc[1]), tA, tB);
    }
  }
  lds_set(myprog, (unsigned)(gK + 2 * w));
  {
    const int i = lane & 15;
    const int koff = (8 * (i >> 2) + (i & 3)) * 256 + 16 * g;
    v4u qf[4];
#pragma unroll 1
    for (int u = w; u < NU; u += 8) {
      lds_wait(full, (unsigned)(gK + 2 * u + 2));
      LAS unsigned char* kb = L + ((gK + 2 * u) % NQ) * 4096 + koff;
      v4u kf[8];
#pragma unroll
      for (int t = 0; t < 2; ++t)
#pragma unroll
        for (int s = 0; s < 4; ++s) kf[t * 4 + s] = ldsr128(kb + t * 1024 + 64 * (s ^ (i & 3)));
      lds_set(myprog, (unsigned)(gK + 2 * (u + 8 < NU ? u + 8 : NU)));
      if (u == w) {
        lds_wait(&flg[F_QRDY], 1u);
#pragma unroll
        for (int s = 0; s < 4; ++s) qf[s] = ldsr128(L + Q_OFF + hd * 256 + 64 * s + 16 * g);
      }
      f32x4 a0 = {0.f, 0.f, 0.f, 0.f}, a1 = {0.f, 0.f, 0.f, 0.f};
#pragma unroll
      for (int s = 0; s < 4; ++s) { a0 = mfma(kf[s], qf[s], a0); a1 = mfma(kf[4 + s], qf[s], a1); }
      const unsigned r0 = pk2(hi8 ? a1[0] : a0[0], hi8 ? a1[1] : a0[1]), r1 = pk2(hi8 ? a1[2] : a0[2], hi8 ? a1[3] : a0[3]);
      const unsigned s0 = pk2(bflo(r0) * 0.08838834764831845f, bfhi(r0) * 0.08838834764831845f);
      const unsigned s1 = pk2(bflo(r1) * 0.08838834764831845f, bfhi(r1) * 0.08838834764831845f);
      const float m = max8rows(fmaxf(fmaxf(bflo(s0), bfhi(s0)), fmaxf(bflo(s1), bfhi(s1))));
      const float e0 = fexp(bflo(s0) - m), e1 = fexp(bfhi(s0) - m), e2 = fexp(bflo(s1) - m), e3 = fexp(bfhi(s1) - m);
      *(LAS v2u*)(L + P_OFF + u * 512 + g * 128 + hd * 16 + (hi8 ? 8 : 0)) = v2u{pk2(e0, e1), pk2(e2, e3)};
      const float ls = sum8rows((e0 + e1) + (e2 + e3));
      if (lane < 8) { ((LAS float*)(L + MU_OFF))[u * 8 + lane] = m; ((LAS float*)(L + LU_OFF))[u * 8 + lane] = ls; }
    }
    lds_set(mystage, 4u);
  }
  {
    lds_wait(stage8, 4u);
    const int uu = lane >> 3;
    const float m0 = ((LAS float*)(L + MU_OFF))[lane], m1 = uu + 8 < NU ? ((LAS float*)(L + MU_OFF))[64 + lane] : -INFINITY, m2 = uu + 16 < NU ? ((LAS float*)(L + MU_OFF))[128 + lane] : -INFINITY;
    const float l0 = ((LAS float*)(L + LU_OFF))[lane], l1 = uu + 8 < NU ? ((LAS float*)(L + LU_OFF))[64 + lane] : 0.f, l2 = uu + 16 < NU ? ((LAS float*)(L + LU_OFF))[128 + lane] : 0.f;
    const float mcu = max8rows(fmaxf(m0, fmaxf(m1, m2)));
    const float lsum = sum8rows(fexp(m0 - mcu) * l0 + fexp(m1 - mcu) * l1 + fexp(m2 - mcu) * l2);
    const float inv = frcp(lsum);
    const float mls = __shfl(mcu, lane >> 3, 64), lls = __shfl(lsum, lane >> 3, 64);
    f32x4 acc = {0.f, 0.f, 0.f, 0.f};
    const int voff = trow * 256 + 32 * (w ^ q4) + 8 * p4;
    LAS unsigned char* pb = L + P_OFF + g * 128 + hd * 16;
    const int gV = gK + 2 * NU;
    int pp = gV % NQ;
#pragma unroll 1
    for (int u = 0; u < NU; ++u) {
      const v4u pf = ldsr128(pb + u * 512);
      const float f = fexp(((LAS float*)(L + MU_OFF))[u * 8 + hd] - mcu) * inv;
      v4u a;
#pragma unroll 1
      for (unsigned it = 1u << 18; it; --it) {
        CBAR();
        const unsigned fv = *full;
        CBAR();
        a = trfrag(L + pp * 4096 + voff, 1024);
        LGKM0();
        if ((unsigned)__builtin_amdgcn_readfirstlane(fv) >= (unsigned)(gV + 2 * u + 2)) break;
        __builtin_amdgcn_s_sleep(1);
      }
      *myprog = (unsigned)(gV + 2 * u + 2);
      const f32x4 t = mfma(a, pf, f32x4{0.f, 0.f, 0.f, 0.f});
      acc[0] = fmaf(f, t[0], acc[0]); acc[1] = fmaf(f, t[1], acc[1]); acc[2] = fmaf(f, t[2], acc[2]); acc[3] = fmaf(f, t[3], acc[3]);
      pp = (pp + 2 == NQ) ? 0 : pp + 2;
    }
    const int src = (lane & 3) * 16 + ((lane >> 2) & 7);
    const unsigned d0 = __shfl(pk2(acc[0], acc[1]), src, 64), d1 = __shfl(pk2(acc[2], acc[3]), src, 64);
    if (lane < 32) gst(rs, WS_AP + (grp * 8 + (lane >> 2)) * AP_STRIDE + sp * 640 + w * 64 + (lane & 3) * 16, d0, d1, tA, tB);
    if (w == 0) gst(rs, WS_AP + (grp * 8 + (lane >> 3)) * AP_STRIDE + sp * 640 + 512 + hd * 16, __float_as_uint(mls), __float_as_uint(lls), tA, tB);
  }
  if (w < 2) {
    v4u v[4];
    const int o = WS_AP + (grp * 8 + (sp >> 1)) * AP_STRIDE + (lane >> 3) * 640;
    const int od = o + (sp & 1) * 256 + w * 128 + hd * 16, om = o + 512 + hd * 16;
    const int off[4] = {od, od + 8 * 640, om, om + 8 * 640};
    poll<4, BO1, FD1>(rs, off, v, tA, tB);
    const float m0 = __uint_as_float(v[2].x), m1 = __uint_as_float(v[3].x);
    const float M = max8rows(fmaxf(m0, m1));
    const float c0 = fexp(m0 - M) * __uint_as_float(v[2].z), c1 = fexp(m1 - M) * __uint_as_float(v[3].z);
    const float inv = frcp(sum8rows(c0 + c1));
    const float a0 = sum8rows(fmaf(c0, bflo(v[0].x), c1 * bflo(v[1].x))) * inv, a1 = sum8rows(fmaf(c0, bfhi(v[0].x), c1 * bfhi(v[1].x))) * inv;
    const float a2 = sum8rows(fmaf(c0, bflo(v[0].z), c1 * bflo(v[1].z))) * inv, a3 = sum8rows(fmaf(c0, bfhi(v[0].z), c1 * bfhi(v[1].z))) * inv;
    const int hl = sp >> 1;
    if (lane < 8) gst(rs, WS_O + (kvh * 4 + (hl >> 1)) * 4096 + bat * 1024 + ((hl & 1) * 32 + (sp & 1) * 16 + w * 8 + lane) * 16, pk2(a0, a1), pk2(a2, a3), tA, tB);
  } else if (heavy && (w == 4 || w == 5)) {
    v4u v[2];
    const int o = WS_GP + mc * 16384 + ks * 4096 + (w - 4) * 2048 + lane * 16;
    const int off[2] = {o, o + 1024};
    poll<2, BO1, 0>(rs, off, v, tA, tB);
    const float x0 = sumrows(__uint_as_float(v[0].x)), w0 = sumrows(__uint_as_float(v[0].z)), x1 = sumrows(__uint_as_float(v[1].x)), w1 = sumrows(__uint_as_float(v[1].z));
    const unsigned gp = bp1 ? pk2(x1, w1) : pk2(x0, w0);
    const unsigned up = __builtin_amdgcn_mov_dpp(gp, 0x128, 0xf, 0xf, true);
    const float g0 = bflo(gp), g1 = bfhi(gp);
    const float z0 = 0.7978845608028654f * (g0 + 0.044715f * g0 * g0 * g0), z1 = 0.7978845608028654f * (g1 + 0.044715f * g1 * g1 * g1);
    const float t0 = 1.f - 2.f * frcp(fexp(2.f * z0) + 1.f), t1 = 1.f - 2.f * frcp(fexp(2.f * z1) + 1.f);
    const unsigned ge = pk2(0.5f * g0 * (1.f + t0), 0.5f * g1 * (1.f + t1));
    const unsigned hp = pk2(bflo(ge) * bflo(up), bfhi(ge) * bfhi(up));
    unsigned d0, d1;
    quad_pack(hp, lane, d0, d1);
    const int src = ((lane >> 1) & 1) * 16 + ((lane >> 2) & 1) * 4 + (lane & 1);
    d0 = __shfl(d0, src, 64); d1 = __shfl(d1, src, 64);
    if (lane < 8) gst(rs, WS_H + ((16 * mc + 4 * ks + 2 * (w - 4)) * 4 + lane) * 16, d0, d1, tA, tB);
  }
  lds_set(mystage, 6u);
  const int gW = gK + 4 * NU;
  lds_wait(&flg[F_STAGE + ((w >= 4 && heavy) ? 4 : 0) + (lane & 1)], 6u);
  lds_wait(full, (unsigned)(gW + 15 - LEADR));
  {
    v4u v[1];
    const int off[1] = {w < 4 ? WS_O + grp * 4096 + ct * 1024 + lane * 16 : WS_H + (224 * grp + min(64 * ct + lane, 223)) * 16};
    poll<1, BO2, FD2>(rs, off, v, tA, tB);
    *(LAS v2u*)(L + (w < 4 ? O_OFF + ct * 512 + lane * 8 : H_OFF + (lane & 3) * 512 + (16 * ct + (lane >> 2)) * 8)) = v2u{v[0].x, v[0].z};
    lds_set(mystage, 7u);
  }
  {
    const int n = lane & 15;
    const int fpoff = WS_FP + ((8 * (w < 4 ? sp : spd) + 2 * ct + (n >> 3)) * 2 + (bp1 ? 1 : 0)) * 4096 + grp * 256 + ((w < 4 ? 0 : 8) + (n & 7)) * 16;
    LAS unsigned char* ab = L + (w < 4 ? O_OFF : H_OFF) + arow * 512 + 16 * g;
    v4u wf[8], afr[8];
    lds_wait(full, (unsigned)(gW + (w < 4 ? 15 : 14)));
#pragma unroll
    for (int k = 0; k < 8; ++k) wf[k] = trfrag(L + ((gW + 2 * k + (w >> 2)) % NQ) * 4096 + t128, 512);
    lds_spin(&flg[F_STAGE + (w & 4) + (lane & 3)], 7u);
#pragma unroll
    for (int k = 0; k < 8; ++k) afr[k] = ldsr128(ab + k * 64);
    f32x4 acc = {0.f, 0.f, 0.f, 0.f}, accb = {0.f, 0.f, 0.f, 0.f};
#pragma unroll
    for (int k = 0; k < 6; k += 2) { acc = mfma(afr[k], wf[k], acc); accb = mfma(afr[k + 1], wf[k + 1], accb); }
    acc = mfma(afr[6], wf[6], acc);
    if (w < 4) accb = mfma(afr[7], wf[7], accb);
    acc += accb;
    if (lane < 32) gst(rs, fpoff, __float_as_uint(bp1 ? acc[2] : acc[0]), __float_as_uint(bp1 ? acc[3] : acc[1]), tA, tB);
  }
  lds_set(mystage, 8u);
}

}

extern "C" void kernel_launch(void* const* d_in, const int* in_sizes, int n_in, void* d_out, int out_size, void* d_ws, size_t ws_size, hipStream_t stream) {
  fk::Params p;
  p.x = (const u16*)d_in[0]; p.kc = (const u16*)d_in[1]; p.vc = (const u16*)d_in[2]; p.wq = (const u16*)d_in[3]; p.wo = (const u16*)d_in[4];
  p.wg = (const u16*)d_in[6]; p.wu = (const u16*)d_in[7]; p.wd = (const u16*)d_in[8];
  p.out = (u16*)d_out; p.ws = (unsigned char*)d_ws;
  (void)hipFuncSetAttribute((const void*)fk::fused_kernel, hipFuncAttributeMaxDynamicSharedMemorySize, fk::LDS_BYTES);
  hipLaunchKernelGGL(fk::fused_kernel, dim3(fk::NCU), dim3(64 * fk::NWAVES), fk::LDS_BYTES, stream, p);
}
```
